```python
import torch

def _softmax(x, axis=-1):
    return torch.softmax(x, dim=axis)
def _gelu(x):
    return torch.nn.functional.gelu(x, approximate="tanh")
def _silu(x):
    return torch.nn.functional.silu(x)
def _sigmoid(x):
    return torch.sigmoid(x)
def _layernorm(x, axis=-1, eps=1e-5):
    m = x.mean(dim=axis, keepdim=True)
    v = x.var(dim=axis, keepdim=True, unbiased=False)
    return (x - m) / torch.sqrt(v + eps)
def _scatter_add(tgt, idx, src, axis=0):
    return tgt.clone().index_add_(axis, idx, src)
def _segment_sum(src, seg, n):
    out = torch.zeros((n,) + src.shape[1:], dtype=src.dtype)
    return out.index_add_(0, seg, src)
def _pad(x, pw):
    flat = tuple(v for pair in reversed(pw) for v in pair)
    return torch.nn.functional.pad(x, flat)

kernel_name = 'concat_heads__linear_attn_block__grouped_kv_mqa__prompt'

def setup_inputs():

    _w = (lambda a: (lambda t: t.to(torch.bfloat16) if t.is_floating_point() else t)(torch.as_tensor(a)))
    def _A_setup():
        B, S, D = 1, 2048, 512
        return {
            "x": _w((lambda *s: torch.randn(*s, dtype=torch.bfloat16) * 0.05)(B, S, D)),
            "wqkv": _w((lambda *s: torch.randn(*s, dtype=torch.bfloat16) * 0.8)(D, 3 * D)),
            "wo": _w((lambda *s: torch.randn(*s, dtype=torch.bfloat16) * 0.05)(D, D)),
        }
    _A_in = _A_setup()

    def _B_setup():
        B, S, D, H = 1, 2048, 512, 32
        Dh = D // H
        return {
            "x": _w((lambda *s: torch.randn(*s, dtype=torch.bfloat16) * 0.05)(B, S, D)),
            "wq": _w((lambda *s: torch.randn(*s, dtype=torch.bfloat16) * 1.6)(D, H * Dh)),
            "wk": _w((lambda *s: torch.randn(*s, dtype=torch.bfloat16) * 1.6)(D, Dh)),
            "wv": _w((lambda *s: torch.randn(*s, dtype=torch.bfloat16) * 0.05)(D, Dh)),
            "wo": _w((lambda *s: torch.randn(*s, dtype=torch.bfloat16) * 0.05)(H * Dh, D)),
        }
    _B_in = _B_setup()

    return {
        "A_x": _A_in["x"],
        "A_wqkv": _A_in["wqkv"],
        "A_wo": _A_in["wo"],
        "B_x": _B_in["x"],
        "B_wq": _B_in["wq"],
        "B_wk": _B_in["wk"],
        "B_wv": _B_in["wv"],
        "B_wo": _B_in["wo"],
    }

def reference(A_x, A_wqkv, A_wo, B_x, B_wq, B_wk, B_wv, B_wo):
    def _A_ref(x, wqkv, wo):
        B, S, D = x.shape
        H = 32; Dh = D // H
        qkv = (lambda x, *d: x.permute(*d))((x @ wqkv).reshape(B, S, 3, H, Dh), 2, 0, 3, 1, 4)
        q, k, v = qkv[0], qkv[1], qkv[2]
        phi = lambda t: torch.where(t > 0, t + 1, torch.exp(t)) + 1e-6
        pq, pk = phi(q), phi(k)
        kv = torch.einsum("bhsd,bhse->bhde", pk, v)
        z = torch.einsum("bhsd->bhd", pk)
        num = torch.einsum("bhqd,bhde->bhqe", pq, kv)
        den = torch.einsum("bhqd,bhd->bhq", pq, z)[..., None] + 1e-6
        o = num / den
        return (lambda x, *d: x.permute(*d))(o, 0, 2, 1, 3).reshape(B, S, D) @ wo

    def _B_ref(x, wq, wk, wv, wo):
        B, S, D = x.shape
        H = 32
        Dh = D // H
        q = (lambda x, *d: x.permute(*d))((x @ wq).reshape(B, S, H, Dh), 0, 2, 1, 3)
        k = x @ wk
        v = x @ wv
        scores = torch.einsum("bhqd,bkd->bhqk", q, k) * (Dh ** -0.5)
        p = _softmax(scores, axis=-1)
        o = torch.einsum("bhqk,bkd->bhqd", p, v)
        return (lambda x, *d: x.permute(*d))(o, 0, 2, 1, 3).reshape(B, S, H * Dh) @ wo

    a = _A_ref(A_x, A_wqkv, A_wo)
    b = _B_ref(B_x, B_wq, B_wk, B_wv, B_wo)
    return torch.concatenate([a, b], axis=-1)
```

```cpp
#include <hip/hip_runtime.h>
#include <hip/hip_bf16.h>
#include <type_traits>

typedef __bf16 bf16x2 __attribute__((ext_vector_type(2)));
typedef __bf16 bf16x8 __attribute__((ext_vector_type(8)));
typedef __bf16 bf16x16 __attribute__((ext_vector_type(16)));
typedef short s16x4 __attribute__((ext_vector_type(4)));
typedef float f32x2 __attribute__((ext_vector_type(2)));
typedef float f32x4 __attribute__((ext_vector_type(4)));
typedef float f32x16 __attribute__((ext_vector_type(16)));
typedef unsigned u32x2 __attribute__((ext_vector_type(2)));
typedef unsigned u32x4 __attribute__((ext_vector_type(4)));
typedef unsigned u32x8 __attribute__((ext_vector_type(8)));
typedef unsigned u32x16 __attribute__((ext_vector_type(16)));
#define LAS __attribute__((address_space(3)))
#define GAS __attribute__((address_space(1)))
#define DEV __device__ __forceinline__
typedef __amdgpu_buffer_rsrc_t rsrc_t;

#define ATT_ASM \
    \
  "v_mov_b32 v32, 0\n" \
  "v_mov_b32 v33, 0\n" \
  "v_mov_b32 v34, 0\n" \
  "v_mov_b32 v35, 0\n" \
  "v_mov_b32 v36, 0\n" \
  "v_mov_b32 v37, 0\n" \
  "v_mov_b32 v38, 0\n" \
  "v_mov_b32 v39, 0\n" \
  "v_mov_b32 v236, 0xdddd\n" \
  "s_mov_b32 s60, 0xffff\n" \
  "s_mov_b32 s61, 0xffff\n" \
  "v_mfma_f32_32x32x8_bf16 v[108:123], v[32:33], v[32:33], 0\n" \
  "v_mov_b32 v76, %15\n" \
  "v_mov_b32 v77, %15\n" \
  "v_mov_b32 v78, %15\n" \
  "v_mov_b32 v79, %15\n" \
  "v_mfma_f32_32x32x8_bf16 v[124:139], v[32:33], v[32:33], 0\n" \
  "v_mov_b32 v80, %15\n" \
  "v_mov_b32 v81, %15\n" \
  "v_mov_b32 v82, %15\n" \
  "v_mov_b32 v83, %15\n" \
  "v_mfma_f32_32x32x8_bf16 v[140:155], v[32:33], v[32:33], 0\n" \
  "v_mov_b32 v88, %15\n" \
  "v_mov_b32 v89, %15\n" \
  "v_mov_b32 v90, %15\n" \
  "v_mov_b32 v91, %15\n" \
  "v_mfma_f32_32x32x8_bf16 v[156:171], v[32:33], v[32:33], 0\n" \
  "v_mov_b32 v92, %15\n" \
  "v_mov_b32 v93, %15\n" \
  "v_mov_b32 v94, %15\n" \
  "v_mov_b32 v95, %15\n" \
  "v_mfma_f32_32x32x8_bf16 v[172:187], v[32:33], v[32:33], 0\n" \
  "v_mov_b32 v100, %15\n" \
  "v_mov_b32 v101, %15\n" \
  "v_mov_b32 v102, %15\n" \
  "v_mov_b32 v103, %15\n" \
  "v_mfma_f32_32x32x8_bf16 v[188:203], v[32:33], v[32:33], 0\n" \
  "v_mov_b32 v104, %15\n" \
  "v_mov_b32 v105, %15\n" \
  "v_mov_b32 v106, %15\n" \
  "v_mov_b32 v107, %15\n" \
  "v_mfma_f32_32x32x8_bf16 v[204:219], v[32:33], v[32:33], 0\n" \
  "v_mfma_f32_32x32x8_bf16 v[220:235], v[32:33], v[32:33], 0\n" \
  "v_xor_b32 v24, 0, %20\n" \
  "v_xor_b32 v25, 32, %20\n" \
  "v_xor_b32 v26, 64, %20\n" \
  "v_xor_b32 v27, 96, %20\n" \
  "v_mov_b32 v28, %21\n" \
  "v_mov_b32 v30, 0x3eb8aa3b\n" \
  "s_waitcnt vmcnt(0)\n" \
  "s_mov_b64 exec, 1\n" \
  "ds_add_u32 %10, %11 offset:64\n" \
  "s_mov_b64 exec, -1\n" \
  "ds_read_b32 v29, %10 offset:64\n" \
  "s_mov_b32 %3, 0x400000\n" \
  "s_branch 71f\n" \
  "70:\n" \
  "s_sub_u32 %3, %3, 1\n" \
  "s_cbranch_scc1 72f\n" \
  "ds_read_b32 v29, %10 offset:64\n" \
  "71:\n" \
  "s_waitcnt lgkmcnt(0)\n" \
  "v_cmp_gt_u32 vcc, 4, v29\n" \
  "s_cbranch_vccnz 70b\n" \
  "72:\n" \
    \
  "ds_read_b128 v[40:43], v24 offset:0\n" \
  "ds_read_b64_tr_b16 v[44:45], v28 offset:0\n" \
  "ds_read_b64_tr_b16 v[46:47], v28 offset:512\n" \
  "ds_read_b128 v[48:51], v25 offset:0\n" \
  "ds_read_b64_tr_b16 v[52:53], v28 offset:2080\n" \
  "ds_read_b64_tr_b16 v[54:55], v28 offset:2592\n" \
  "ds_read_b128 v[56:59], v26 offset:0\n" \
  "ds_read_b64_tr_b16 v[60:61], v28 offset:4160\n" \
  "ds_read_b64_tr_b16 v[62:63], v28 offset:4672\n" \
  "ds_read_b128 v[64:67], v27 offset:0\n" \
  "ds_read_b64_tr_b16 v[68:69], v28 offset:6240\n" \
  "ds_read_b64_tr_b16 v[70:71], v28 offset:6752\n" \
  "s_waitcnt lgkmcnt(9)\n" \
  "v_mfma_f32_32x32x16_bf16 v[8:23], v[44:47], v[40:43], 0\n" \
  "ds_read_b128 v[40:43], v24 offset:8320\n" \
  "ds_read_b64_tr_b16 v[44:45], v28 offset:8320\n" \
  "ds_read_b64_tr_b16 v[46:47], v28 offset:8832\n" \
  "s_waitcnt lgkmcnt(9)\n" \
  "v_mfma_f32_32x32x16_bf16 v[8:23], v[52:55], v[48:51], v[8:23]\n" \
  "ds_read_b128 v[48:51], v25 offset:8320\n" \
  "ds_read_b64_tr_b16 v[52:53], v28 offset:10400\n" \
  "ds_read_b64_tr_b16 v[54:55], v28 offset:10912\n" \
  "s_waitcnt lgkmcnt(9)\n" \
  "v_mfma_f32_32x32x16_bf16 v[8:23], v[60:63], v[56:59], v[8:23]\n" \
  "ds_read_b128 v[56:59], v26 offset:8320\n" \
  "ds_read_b64_tr_b16 v[60:61], v28 offset:12480\n" \
  "ds_read_b64_tr_b16 v[62:63], v28 offset:12992\n" \
  "s_waitcnt lgkmcnt(9)\n" \
  "v_mfma_f32_32x32x16_bf16 v[8:23], v[68:71], v[64:67], v[8:23]\n" \
  "ds_read_b128 v[64:67], v27 offset:8320\n" \
  "ds_read_b64_tr_b16 v[68:69], v28 offset:14560\n" \
  "ds_read_b64_tr_b16 v[70:71], v28 offset:15072\n" \
  "s_waitcnt lgkmcnt(9)\n" \
  "v_mfma_f32_32x32x16_bf16 v[8:23], v[44:47], v[40:43], v[8:23]\n" \
  "ds_read_b128 v[40:43], v24 offset:33280\n" \
  "ds_read_b64_tr_b16 v[44:45], v28 offset:33280\n" \
  "ds_read_b64_tr_b16 v[46:47], v28 offset:33792\n" \
  "s_waitcnt lgkmcnt(9)\n" \
  "v_mfma_f32_32x32x16_bf16 v[8:23], v[52:55], v[48:51], v[8:23]\n" \
  "ds_read_b128 v[48:51], v25 offset:33280\n" \
  "ds_read_b64_tr_b16 v[52:53], v28 offset:35360\n" \
  "ds_read_b64_tr_b16 v[54:55], v28 offset:35872\n" \
  "s_waitcnt lgkmcnt(9)\n" \
  "v_mfma_f32_32x32x16_bf16 v[8:23], v[60:63], v[56:59], v[8:23]\n" \
  "ds_read_b128 v[56:59], v26 offset:33280\n" \
  "ds_read_b64_tr_b16 v[60:61], v28 offset:37440\n" \
  "ds_read_b64_tr_b16 v[62:63], v28 offset:37952\n" \
  "s_waitcnt lgkmcnt(9)\n" \
  "v_mfma_f32_32x32x16_bf16 v[8:23], v[68:71], v[64:67], v[8:23]\n" \
  "ds_read_b128 v[64:67], v27 offset:33280\n" \
  "ds_read_b64_tr_b16 v[68:69], v28 offset:39520\n" \
  "ds_read_b64_tr_b16 v[70:71], v28 offset:40032\n" \
  "s_waitcnt lgkmcnt(9)\n" \
  "v_mfma_f32_32x32x16_bf16 v[8:23], v[44:47], v[40:43], v[8:23]\n" \
  "ds_read_b32 v29, %10 offset:68\n" \
  "ds_read_b128 v[40:43], v24 offset:41600\n" \
  "ds_read_b64_tr_b16 v[44:45], v28 offset:41600\n" \
  "ds_read_b64_tr_b16 v[46:47], v28 offset:42112\n" \
  "s_waitcnt lgkmcnt(10)\n" \
  "v_mfma_f32_32x32x16_bf16 v[8:23], v[52:55], v[48:51], v[8:23]\n" \
  "ds_read_b128 v[48:51], v25 offset:41600\n" \
  "ds_read_b64_tr_b16 v[52:53], v28 offset:43680\n" \
  "ds_read_b64_tr_b16 v[54:55], v28 offset:44192\n" \
  "s_waitcnt lgkmcnt(10)\n" \
  "v_mfma_f32_32x32x16_bf16 v[8:23], v[60:63], v[56:59], v[8:23]\n" \
  "ds_read_b128 v[56:59], v26 offset:41600\n" \
  "ds_read_b64_tr_b16 v[60:61], v28 offset:45760\n" \
  "ds_read_b64_tr_b16 v[62:63], v28 offset:46272\n" \
  "s_waitcnt lgkmcnt(10)\n" \
  "v_mfma_f32_32x32x16_bf16 v[8:23], v[68:71], v[64:67], v[8:23]\n" \
  "ds_read_b128 v[64:67], v27 offset:41600\n" \
  "ds_read_b64_tr_b16 v[68:69], v28 offset:47840\n" \
  "ds_read_b64_tr_b16 v[70:71], v28 offset:48352\n" \
  "s_waitcnt lgkmcnt(9)\n" \
  "v_mfma_f32_32x32x16_bf16 v[8:23], v[44:47], v[40:43], v[8:23]\n" \
  "v_cmp_gt_u32 vcc, 3, v29\n" \
  "s_cbranch_vccnz 74f\n" \
  "75:\n" \
  "v_add_u32 v24, 66560, v24\n" \
  "v_add_u32 v25, 66560, v25\n" \
  "v_add_u32 v26, 66560, v26\n" \
  "v_add_u32 v27, 66560, v27\n" \
  "v_add_u32 v28, 66560, v28\n" \
  "ds_read_b128 v[40:43], v24 offset:0\n" \
  "ds_read_b64_tr_b16 v[44:45], v28 offset:0\n" \
  "ds_read_b64_tr_b16 v[46:47], v28 offset:512\n" \
  "s_waitcnt lgkmcnt(9)\n" \
  "v_mfma_f32_32x32x16_bf16 v[8:23], v[52:55], v[48:51], v[8:23]\n" \
  "ds_read_b128 v[48:51], v25 offset:0\n" \
  "ds_read_b64_tr_b16 v[52:53], v28 offset:2080\n" \
  "ds_read_b64_tr_b16 v[54:55], v28 offset:2592\n" \
  "s_waitcnt lgkmcnt(9)\n" \
  "v_mfma_f32_32x32x16_bf16 v[8:23], v[60:63], v[56:59], v[8:23]\n" \
  "ds_read_b128 v[56:59], v26 offset:0\n" \
  "ds_read_b64_tr_b16 v[60:61], v28 offset:4160\n" \
  "ds_read_b64_tr_b16 v[62:63], v28 offset:4672\n" \
  "s_waitcnt lgkmcnt(9)\n" \
  "v_mfma_f32_32x32x16_bf16 v[8:23], v[68:71], v[64:67], v[8:23]\n" \
  "ds_read_b128 v[64:67], v27 offset:0\n" \
  "ds_read_b64_tr_b16 v[68:69], v28 offset:6240\n" \
  "ds_read_b64_tr_b16 v[70:71], v28 offset:6752\n" \
  "s_waitcnt lgkmcnt(9)\n" \
  "v_mfma_f32_32x32x16_bf16 v[8:23], v[44:47], v[40:43], v[8:23]\n" \
  "ds_read_b128 v[40:43], v24 offset:8320\n" \
  "ds_read_b64_tr_b16 v[44:45], v28 offset:8320\n" \
  "ds_read_b64_tr_b16 v[46:47], v28 offset:8832\n" \
  "s_waitcnt lgkmcnt(9)\n" \
  "v_mfma_f32_32x32x16_bf16 v[8:23], v[52:55], v[48:51], v[8:23]\n" \
  "ds_read_b128 v[48:51], v25 offset:8320\n" \
  "ds_read_b64_tr_b16 v[52:53], v28 offset:10400\n" \
  "ds_read_b64_tr_b16 v[54:55], v28 offset:10912\n" \
  "s_waitcnt lgkmcnt(9)\n" \
  "v_mfma_f32_32x32x16_bf16 v[8:23], v[60:63], v[56:59], v[8:23]\n" \
  "ds_read_b128 v[56:59], v26 offset:8320\n" \
  "ds_read_b64_tr_b16 v[60:61], v28 offset:12480\n" \
  "ds_read_b64_tr_b16 v[62:63], v28 offset:12992\n" \
  "s_waitcnt lgkmcnt(9)\n" \
  "v_mfma_f32_32x32x16_bf16 v[8:23], v[68:71], v[64:67], v[8:23]\n" \
  "ds_read_b128 v[64:67], v27 offset:8320\n" \
  "ds_read_b64_tr_b16 v[68:69], v28 offset:14560\n" \
  "ds_read_b64_tr_b16 v[70:71], v28 offset:15072\n" \
  "s_waitcnt lgkmcnt(9)\n" \
  "v_mfma_f32_32x32x16_bf16 v[8:23], v[44:47], v[40:43], v[8:23]\n" \
  "ds_read_b128 v[40:43], v24 offset:33280\n" \
  "ds_read_b64_tr_b16 v[44:45], v28 offset:33280\n" \
  "ds_read_b64_tr_b16 v[46:47], v28 offset:33792\n" \
  "s_waitcnt lgkmcnt(9)\n" \
  "v_mfma_f32_32x32x16_bf16 v[8:23], v[52:55], v[48:51], v[8:23]\n" \
  "ds_read_b128 v[48:51], v25 offset:33280\n" \
  "ds_read_b64_tr_b16 v[52:53], v28 offset:35360\n" \
  "ds_read_b64_tr_b16 v[54:55], v28 offset:35872\n" \
  "s_waitcnt lgkmcnt(9)\n" \
  "v_mfma_f32_32x32x16_bf16 v[8:23], v[60:63], v[56:59], v[8:23]\n" \
  "ds_read_b128 v[56:59], v26 offset:33280\n" \
  "ds_read_b64_tr_b16 v[60:61], v28 offset:37440\n" \
  "ds_read_b64_tr_b16 v[62:63], v28 offset:37952\n" \
  "s_waitcnt lgkmcnt(9)\n" \
  "v_mfma_f32_32x32x16_bf16 v[8:23], v[68:71], v[64:67], v[8:23]\n" \
  "ds_read_b128 v[64:67], v27 offset:33280\n" \
  "ds_read_b64_tr_b16 v[68:69], v28 offset:39520\n" \
  "ds_read_b64_tr_b16 v[70:71], v28 offset:40032\n" \
  "s_waitcnt lgkmcnt(9)\n" \
  "v_mfma_f32_32x32x16_bf16 v[8:23], v[44:47], v[40:43], v[8:23]\n" \
  "ds_read_b128 v[40:43], v24 offset:41600\n" \
  "ds_read_b64_tr_b16 v[44:45], v28 offset:41600\n" \
  "ds_read_b64_tr_b16 v[46:47], v28 offset:42112\n" \
  "s_waitcnt lgkmcnt(9)\n" \
  "v_mfma_f32_32x32x16_bf16 v[8:23], v[52:55], v[48:51], v[8:23]\n" \
  "ds_read_b128 v[48:51], v25 offset:41600\n" \
  "ds_read_b64_tr_b16 v[52:53], v28 offset:43680\n" \
  "ds_read_b64_tr_b16 v[54:55], v28 offset:44192\n" \
  "s_waitcnt lgkmcnt(9)\n" \
  "v_mfma_f32_32x32x16_bf16 v[8:23], v[60:63], v[56:59], v[8:23]\n" \
  "ds_read_b128 v[56:59], v26 offset:41600\n" \
  "ds_read_b64_tr_b16 v[60:61], v28 offset:45760\n" \
  "ds_read_b64_tr_b16 v[62:63], v28 offset:46272\n" \
  "s_waitcnt lgkmcnt(9)\n" \
  "v_mfma_f32_32x32x16_bf16 v[8:23], v[68:71], v[64:67], v[8:23]\n" \
  "ds_read_b128 v[64:67], v27 offset:41600\n" \
  "ds_read_b64_tr_b16 v[68:69], v28 offset:47840\n" \
  "ds_read_b64_tr_b16 v[70:71], v28 offset:48352\n" \
  "ds_read_b32 v29, %14 offset:16\n" \
  "s_waitcnt lgkmcnt(10)\n" \
  "v_mfma_f32_32x32x16_bf16 v[8:23], v[44:47], v[40:43], v[8:23]\n" \
  "s_waitcnt lgkmcnt(7)\n" \
  "v_mfma_f32_32x32x16_bf16 v[8:23], v[52:55], v[48:51], v[8:23]\n" \
  "s_waitcnt lgkmcnt(4)\n" \
  "v_mfma_f32_32x32x16_bf16 v[8:23], v[60:63], v[56:59], v[8:23]\n" \
  "s_waitcnt lgkmcnt(1)\n" \
  "v_mfma_f32_32x32x16_bf16 v[8:23], v[68:71], v[64:67], v[8:23]\n" \
    \
  "s_waitcnt lgkmcnt(0)\n" \
  "v_cmp_gt_u32 vcc, 1, v29\n" \
  "s_cbranch_vccnz 77f\n" \
  "buffer_load_dwordx4 v[72:75], %7, %9, %2 offen offset:0 sc1\n" \
  "s_mov_b64 exec, s[60:61]\n" \
  "buffer_load_dwordx4 v[76:79], %8, %9, %2 offen offset:1024 sc1\n" \
  "buffer_load_dwordx4 v[80:83], %8, %9, %2 offen offset:1536 sc1\n" \
  "s_mov_b64 exec, -1\n" \
  "77:\n" \
  "v_xor_b32 v24, 2048, %12\n" \
  "v_xor_b32 v25, 4096, %12\n" \
  "v_xor_b32 v26, 6144, %12\n" \
  "s_nop 5\n" \
    \
  "v_cvt_pk_bf16_f32 v8, 0, v8\n" \
  "v_cvt_pk_bf16_f32 v9, 0, v9\n" \
  "v_cvt_pk_bf16_f32 v10, 0, v10\n" \
  "v_cvt_pk_bf16_f32 v11, 0, v11\n" \
  "v_cvt_pk_bf16_f32 v12, 0, v12\n" \
  "v_cvt_pk_bf16_f32 v13, 0, v13\n" \
  "v_cvt_pk_bf16_f32 v14, 0, v14\n" \
  "v_cvt_pk_bf16_f32 v15, 0, v15\n" \
  "v_cvt_pk_bf16_f32 v16, 0, v16\n" \
  "v_cvt_pk_bf16_f32 v17, 0, v17\n" \
  "v_cvt_pk_bf16_f32 v18, 0, v18\n" \
  "v_cvt_pk_bf16_f32 v19, 0, v19\n" \
  "v_cvt_pk_bf16_f32 v20, 0, v20\n" \
  "v_cvt_pk_bf16_f32 v21, 0, v21\n" \
  "v_cvt_pk_bf16_f32 v22, 0, v22\n" \
  "v_cvt_pk_bf16_f32 v23, 0, v23\n" \
  ".long 0xd3b14008, 0x08023d08\n" \
  ".long 0xd3b1400a, 0x08023d0a\n" \
  ".long 0xd3b1400c, 0x08023d0c\n" \
  ".long 0xd3b1400e, 0x08023d0e\n" \
  ".long 0xd3b14010, 0x08023d10\n" \
  ".long 0xd3b14012, 0x08023d12\n" \
  ".long 0xd3b14014, 0x08023d14\n" \
  ".long 0xd3b14016, 0x08023d16\n" \
  "v_cvt_pk_bf16_f32 v64, v8, v9\n" \
  "v_cvt_pk_bf16_f32 v65, v10, v11\n" \
  "v_cvt_pk_bf16_f32 v66, v12, v13\n" \
  "v_cvt_pk_bf16_f32 v67, v14, v15\n" \
  "v_cvt_pk_bf16_f32 v68, v16, v17\n" \
  "v_cvt_pk_bf16_f32 v69, v18, v19\n" \
  "v_cvt_pk_bf16_f32 v70, v20, v21\n" \
  "v_cvt_pk_bf16_f32 v71, v22, v23\n" \
  "ds_write_b128 %12, v[64:67]\n" \
  "ds_write_b128 %12, v[68:71] offset:1024\n" \
  "s_mov_b64 exec, 1\n" \
  "ds_add_u32 %10, %11 offset:0\n" \
  "s_mov_b64 exec, -1\n" \
  "s_cbranch_vccz 78f\n" \
  "ds_read_b32 v29, %14 offset:16\n" \
  "s_mov_b32 %3, 0x400000\n" \
  "s_branch 3f\n" \
  "2:\n" \
  "s_sub_u32 %3, %3, 1\n" \
  "s_cbranch_scc1 4f\n" \
  "ds_read_b32 v29, %14 offset:16\n" \
  "3:\n" \
  "s_waitcnt lgkmcnt(0)\n" \
  "v_cmp_gt_u32 vcc, 1, v29\n" \
  "s_cbranch_vccnz 2b\n" \
  "4:\n" \
  "buffer_load_dwordx4 v[72:75], %7, %9, %2 offen offset:0 sc1\n" \
  "s_mov_b64 exec, s[60:61]\n" \
  "buffer_load_dwordx4 v[76:79], %8, %9, %2 offen offset:1024 sc1\n" \
  "buffer_load_dwordx4 v[80:83], %8, %9, %2 offen offset:1536 sc1\n" \
  "s_mov_b64 exec, -1\n" \
  "78:\n" \
  "ds_read_b32 v29, %10 offset:0\n" \
  "s_mov_b32 %3, 0x400000\n" \
  "s_branch 6f\n" \
  "5:\n" \
  "s_sub_u32 %3, %3, 1\n" \
  "s_cbranch_scc1 7f\n" \
  "ds_read_b32 v29, %10 offset:0\n" \
  "6:\n" \
  "s_waitcnt lgkmcnt(0)\n" \
  "v_cmp_gt_u32 vcc, 5, v29\n" \
  "s_cbranch_vccnz 5b\n" \
  "7:\n" \
  "ds_read_b128 v[40:43], v24 offset:0\n" \
  "ds_read_b128 v[44:47], v24 offset:1024\n" \
  "ds_read_b128 v[48:51], v25 offset:0\n" \
  "ds_read_b128 v[52:55], v25 offset:1024\n" \
  "ds_read_b128 v[56:59], v26 offset:0\n" \
  "ds_read_b128 v[60:63], v26 offset:1024\n" \
  "s_add_u32 %2, %2, 14336\n" \
  "s_xor_b32 %4, %4, %16\n" \
  "s_mov_b32 %3, 4\n" \
  "s_waitcnt vmcnt(2) lgkmcnt(5)\n" \
  "v_mfma_f32_32x32x16_bf16 v[8:23], v[72:75], v[40:43], 0\n" \
  "s_waitcnt vmcnt(0) lgkmcnt(0)\n" \
  "s_nop 9\n" \
  "s_branch 10f\n" \
    \
  "1:\n" \
  "v_exp_f32 v34, v34\n" \
  "v_exp_f32 v35, v35\n" \
  "v_exp_f32 v36, v36\n" \
  "v_exp_f32 v37, v37\n" \
  "v_exp_f32 v38, v38\n" \
  "v_exp_f32 v39, v39\n" \
  "10:\n" \
    \
  "v_exp_f32 v8, v8\n" \
  "v_exp_f32 v9, v9\n" \
  "v_smfmac_f32_32x32x32_bf16 v[220:235], v[104:107], v[32:39], v236\n" \
  "s_setprio 2\n" \
  "v_exp_f32 v10, v10\n" \
  "v_exp_f32 v11, v11\n" \
  "v_exp_f32 v12, v12\n" \
  "v_mfma_f32_32x32x16_bf16 v[24:39], v[72:75], v[44:47], 0\n" \
  "s_nop 0\n" \
  "v_exp_f32 v13, v13\n" \
  "v_exp_f32 v14, v14\n" \
  "v_exp_f32 v15, v15\n" \
  "v_exp_f32 v16, v16\n" \
  "v_exp_f32 v17, v17\n" \
  "v_smfmac_f32_32x32x32_bf16 v[108:123], v[76:79], v[8:15], v236\n" \
  "s_setprio 0\n" \
  "v_exp_f32 v18, v18\n" \
  "v_exp_f32 v19, v19\n" \
  "v_exp_f32 v20, v20\n" \
  "v_exp_f32 v21, v21\n" \
  "v_exp_f32 v22, v22\n" \
  "v_exp_f32 v23, v23\n" \
  "v_exp_f32 v24, v24\n" \
  "v_exp_f32 v25, v25\n" \
  "v_smfmac_f32_32x32x32_bf16 v[108:123], v[80:83], v[16:23], v236\n" \
  "s_setprio 2\n" \
  "v_exp_f32 v26, v26\n" \
  "v_exp_f32 v27, v27\n" \
  "v_exp_f32 v28, v28\n" \
  "v_mfma_f32_32x32x16_bf16 v[8:23], v[72:75], v[48:51], 0\n" \
  "s_nop 0\n" \
  "v_exp_f32 v29, v29\n" \
  "v_exp_f32 v30, v30\n" \
  "v_exp_f32 v31, v31\n" \
  "v_exp_f32 v32, v32\n" \
  "v_exp_f32 v33, v33\n" \
  "v_smfmac_f32_32x32x32_bf16 v[124:139], v[76:79], v[24:31], v236\n" \
  "s_setprio 0\n" \
  "v_exp_f32 v34, v34\n" \
  "v_exp_f32 v35, v35\n" \
  "v_exp_f32 v36, v36\n" \
  "v_exp_f32 v37, v37\n" \
  "v_exp_f32 v38, v38\n" \
  "v_exp_f32 v39, v39\n" \
  "v_exp_f32 v8, v8\n" \
  "v_exp_f32 v9, v9\n" \
  "v_smfmac_f32_32x32x32_bf16 v[124:139], v[80:83], v[32:39], v236\n" \
  "s_setprio 2\n" \
  "v_exp_f32 v10, v10\n" \
  "v_exp_f32 v11, v11\n" \
  "v_exp_f32 v12, v12\n" \
  "v_mfma_f32_32x32x16_bf16 v[24:39], v[72:75], v[52:55], 0\n" \
  "s_nop 0\n" \
  "v_exp_f32 v13, v13\n" \
  "v_exp_f32 v14, v14\n" \
  "v_exp_f32 v15, v15\n" \
  "v_exp_f32 v16, v16\n" \
  "v_exp_f32 v17, v17\n" \
  "v_smfmac_f32_32x32x32_bf16 v[140:155], v[76:79], v[8:15], v236\n" \
  "s_setprio 0\n" \
  "v_exp_f32 v18, v18\n" \
  "v_exp_f32 v19, v19\n" \
  "v_exp_f32 v20, v20\n" \
  "v_exp_f32 v21, v21\n" \
  "v_exp_f32 v22, v22\n" \
  "v_exp_f32 v23, v23\n" \
  "v_exp_f32 v24, v24\n" \
  "v_exp_f32 v25, v25\n" \
  "v_smfmac_f32_32x32x32_bf16 v[140:155], v[80:83], v[16:23], v236\n" \
  "s_setprio 2\n" \
  "v_exp_f32 v26, v26\n" \
  "v_exp_f32 v27, v27\n" \
  "v_exp_f32 v28, v28\n" \
  "v_mfma_f32_32x32x16_bf16 v[8:23], v[72:75], v[56:59], 0\n" \
  "s_nop 0\n" \
  "v_exp_f32 v29, v29\n" \
  "v_exp_f32 v30, v30\n" \
  "v_exp_f32 v31, v31\n" \
  "v_exp_f32 v32, v32\n" \
  "v_exp_f32 v33, v33\n" \
  "v_smfmac_f32_32x32x32_bf16 v[156:171], v[76:79], v[24:31], v236\n" \
  "s_setprio 0\n" \
  "v_exp_f32 v34, v34\n" \
  "v_exp_f32 v35, v35\n" \
  "v_exp_f32 v36, v36\n" \
  "v_exp_f32 v37, v37\n" \
  "v_exp_f32 v38, v38\n" \
  "v_exp_f32 v39, v39\n" \
  "v_exp_f32 v8, v8\n" \
  "v_exp_f32 v9, v9\n" \
  "v_smfmac_f32_32x32x32_bf16 v[156:171], v[80:83], v[32:39], v236\n" \
  "s_setprio 2\n" \
  "ds_read_b32 v84, %14 offset:16\n" \
  "v_exp_f32 v10, v10\n" \
  "v_exp_f32 v11, v11\n" \
  "v_exp_f32 v12, v12\n" \
  "v_mfma_f32_32x32x16_bf16 v[24:39], v[72:75], v[60:63], 0\n" \
  "s_nop 0\n" \
  "v_exp_f32 v13, v13\n" \
  "v_exp_f32 v14, v14\n" \
  "v_exp_f32 v15, v15\n" \
  "v_exp_f32 v16, v16\n" \
  "v_exp_f32 v17, v17\n" \
  "v_smfmac_f32_32x32x32_bf16 v[172:187], v[76:79], v[8:15], v236\n" \
  "s_setprio 0\n" \
  "s_waitcnt vmcnt(0) lgkmcnt(0)\n" \
  "v_cmp_gt_u32 vcc, 2, v84\n" \
  "s_cbranch_vccnz 50f\n" \
  "51:\n" \
  "v_exp_f32 v18, v18\n" \
  "v_exp_f32 v19, v19\n" \
  "v_exp_f32 v20, v20\n" \
  "v_exp_f32 v21, v21\n" \
  "v_exp_f32 v22, v22\n" \
  "v_exp_f32 v23, v23\n" \
  "v_exp_f32 v24, v24\n" \
  "v_exp_f32 v25, v25\n" \
  "v_smfmac_f32_32x32x32_bf16 v[172:187], v[80:83], v[16:23], v236\n" \
  "s_setprio 2\n" \
  "v_exp_f32 v26, v26\n" \
  "v_exp_f32 v27, v27\n" \
  "v_exp_f32 v28, v28\n" \
  "v_mfma_f32_32x32x16_bf16 v[8:23], v[72:75], v[64:67], 0\n" \
  "ds_read_b128 v[84:87], %5 offset:0\n" \
  "v_exp_f32 v29, v29\n" \
  "v_exp_f32 v30, v30\n" \
  "v_exp_f32 v31, v31\n" \
  "v_exp_f32 v32, v32\n" \
  "v_exp_f32 v33, v33\n" \
  "v_smfmac_f32_32x32x32_bf16 v[188:203], v[76:79], v[24:31], v236\n" \
  "s_setprio 0\n" \
  "s_mov_b64 exec, s[60:61]\n" \
  "ds_read_b128 v[88:91], %6 offset:1024\n" \
  "ds_read_b128 v[92:95], %6 offset:1536\n" \
  "s_mov_b64 exec, -1\n" \
  "v_exp_f32 v34, v34\n" \
  "v_exp_f32 v35, v35\n" \
  "v_exp_f32 v36, v36\n" \
  "v_exp_f32 v37, v37\n" \
  "v_exp_f32 v38, v38\n" \
  "v_exp_f32 v39, v39\n" \
  "v_exp_f32 v8, v8\n" \
  "v_exp_f32 v9, v9\n" \
  "v_smfmac_f32_32x32x32_bf16 v[188:203], v[80:83], v[32:39], v236\n" \
  "s_setprio 2\n" \
  "v_exp_f32 v10, v10\n" \
  "v_exp_f32 v11, v11\n" \
  "v_exp_f32 v12, v12\n" \
  "v_mfma_f32_32x32x16_bf16 v[24:39], v[72:75], v[68:71], 0\n" \
  "s_nop 0\n" \
  "v_exp_f32 v13, v13\n" \
  "v_exp_f32 v14, v14\n" \
  "v_exp_f32 v15, v15\n" \
  "v_exp_f32 v16, v16\n" \
  "v_exp_f32 v17, v17\n" \
  "v_smfmac_f32_32x32x32_bf16 v[204:219], v[76:79], v[8:15], v236\n" \
  "s_setprio 0\n" \
  "v_exp_f32 v18, v18\n" \
  "v_exp_f32 v19, v19\n" \
  "v_exp_f32 v20, v20\n" \
  "v_exp_f32 v21, v21\n" \
  "v_exp_f32 v22, v22\n" \
  "v_exp_f32 v23, v23\n" \
  "v_exp_f32 v24, v24\n" \
  "v_exp_f32 v25, v25\n" \
  "v_smfmac_f32_32x32x32_bf16 v[204:219], v[80:83], v[16:23], v236\n" \
  "s_setprio 2\n" \
  "s_waitcnt lgkmcnt(0)\n" \
  "v_exp_f32 v26, v26\n" \
  "v_exp_f32 v27, v27\n" \
  "v_exp_f32 v28, v28\n" \
  "v_mfma_f32_32x32x16_bf16 v[8:23], v[84:87], v[40:43], 0\n" \
  "s_nop 0\n" \
  "v_exp_f32 v29, v29\n" \
  "v_exp_f32 v30, v30\n" \
  "v_exp_f32 v31, v31\n" \
  "v_exp_f32 v32, v32\n" \
  "v_exp_f32 v33, v33\n" \
  "v_smfmac_f32_32x32x32_bf16 v[220:235], v[76:79], v[24:31], v236\n" \
  "s_setprio 0\n" \
  "v_exp_f32 v34, v34\n" \
  "v_exp_f32 v35, v35\n" \
  "v_exp_f32 v36, v36\n" \
  "v_exp_f32 v37, v37\n" \
  "v_exp_f32 v38, v38\n" \
  "v_exp_f32 v39, v39\n" \
    \
  "v_exp_f32 v8, v8\n" \
  "v_exp_f32 v9, v9\n" \
  "v_smfmac_f32_32x32x32_bf16 v[220:235], v[80:83], v[32:39], v236\n" \
  "s_setprio 2\n" \
  "s_sub_u32 %3, %3, 1\n" \
  "v_exp_f32 v10, v10\n" \
  "v_exp_f32 v11, v11\n" \
  "v_exp_f32 v12, v12\n" \
  "v_mfma_f32_32x32x16_bf16 v[24:39], v[84:87], v[44:47], 0\n" \
  "s_cmp_lt_u32 %3, 3\n" \
  "v_exp_f32 v13, v13\n" \
  "v_exp_f32 v14, v14\n" \
  "v_exp_f32 v15, v15\n" \
  "v_exp_f32 v16, v16\n" \
  "v_exp_f32 v17, v17\n" \
  "v_smfmac_f32_32x32x32_bf16 v[108:123], v[88:91], v[8:15], v236\n" \
  "s_setprio 0\n" \
  "s_cselect_b64 vcc, -1, 0\n" \
  "v_exp_f32 v18, v18\n" \
  "v_exp_f32 v19, v19\n" \
  "v_exp_f32 v20, v20\n" \
  "v_exp_f32 v21, v21\n" \
  "v_exp_f32 v22, v22\n" \
  "v_exp_f32 v23, v23\n" \
  "v_exp_f32 v24, v24\n" \
  "v_exp_f32 v25, v25\n" \
  "v_smfmac_f32_32x32x32_bf16 v[108:123], v[92:95], v[16:23], v236\n" \
  "s_setprio 2\n" \
  "s_mov_b32 m0, %4\n" \
  "v_exp_f32 v26, v26\n" \
  "v_exp_f32 v27, v27\n" \
  "v_exp_f32 v28, v28\n" \
  "v_mfma_f32_32x32x16_bf16 v[8:23], v[84:87], v[48:51], 0\n" \
  "s_nop 0\n" \
  "v_exp_f32 v29, v29\n" \
  "v_exp_f32 v30, v30\n" \
  "v_exp_f32 v31, v31\n" \
  "v_exp_f32 v32, v32\n" \
  "v_exp_f32 v33, v33\n" \
  "v_smfmac_f32_32x32x32_bf16 v[124:139], v[88:91], v[24:31], v236\n" \
  "s_setprio 0\n" \
  "v_exp_f32 v34, v34\n" \
  "v_exp_f32 v35, v35\n" \
  "v_exp_f32 v36, v36\n" \
  "v_exp_f32 v37, v37\n" \
  "v_exp_f32 v38, v38\n" \
  "v_exp_f32 v39, v39\n" \
  "v_exp_f32 v8, v8\n" \
  "v_exp_f32 v9, v9\n" \
  "v_smfmac_f32_32x32x32_bf16 v[124:139], v[92:95], v[32:39], v236\n" \
  "s_setprio 2\n" \
  "v_exp_f32 v10, v10\n" \
  "v_exp_f32 v11, v11\n" \
  "v_exp_f32 v12, v12\n" \
  "v_mfma_f32_32x32x16_bf16 v[24:39], v[84:87], v[52:55], 0\n" \
  "s_nop 0\n" \
  "v_exp_f32 v13, v13\n" \
  "v_exp_f32 v14, v14\n" \
  "v_exp_f32 v15, v15\n" \
  "v_exp_f32 v16, v16\n" \
  "v_exp_f32 v17, v17\n" \
  "v_smfmac_f32_32x32x32_bf16 v[140:155], v[88:91], v[8:15], v236\n" \
  "s_setprio 0\n" \
  "v_exp_f32 v18, v18\n" \
  "v_exp_f32 v19, v19\n" \
  "v_exp_f32 v20, v20\n" \
  "v_exp_f32 v21, v21\n" \
  "v_exp_f32 v22, v22\n" \
  "v_exp_f32 v23, v23\n" \
  "v_exp_f32 v24, v24\n" \
  "v_exp_f32 v25, v25\n" \
  "v_smfmac_f32_32x32x32_bf16 v[140:155], v[92:95], v[16:23], v236\n" \
  "s_setprio 2\n" \
  "v_exp_f32 v26, v26\n" \
  "v_exp_f32 v27, v27\n" \
  "v_exp_f32 v28, v28\n" \
  "v_mfma_f32_32x32x16_bf16 v[8:23], v[84:87], v[56:59], 0\n" \
  "s_nop 0\n" \
  "v_exp_f32 v29, v29\n" \
  "v_exp_f32 v30, v30\n" \
  "v_exp_f32 v31, v31\n" \
  "v_exp_f32 v32, v32\n" \
  "v_exp_f32 v33, v33\n" \
  "v_smfmac_f32_32x32x32_bf16 v[156:171], v[88:91], v[24:31], v236\n" \
  "s_setprio 0\n" \
  "v_exp_f32 v34, v34\n" \
  "v_exp_f32 v35, v35\n" \
  "v_exp_f32 v36, v36\n" \
  "v_exp_f32 v37, v37\n" \
  "v_exp_f32 v38, v38\n" \
  "v_exp_f32 v39, v39\n" \
  "v_exp_f32 v8, v8\n" \
  "v_exp_f32 v9, v9\n" \
  "v_smfmac_f32_32x32x32_bf16 v[156:171], v[92:95], v[32:39], v236\n" \
  "s_setprio 2\n" \
  "v_exp_f32 v10, v10\n" \
  "v_exp_f32 v11, v11\n" \
  "v_exp_f32 v12, v12\n" \
  "v_mfma_f32_32x32x16_bf16 v[24:39], v[84:87], v[60:63], 0\n" \
  "s_nop 0\n" \
  "v_exp_f32 v13, v13\n" \
  "v_exp_f32 v14, v14\n" \
  "v_exp_f32 v15, v15\n" \
  "v_exp_f32 v16, v16\n" \
  "v_exp_f32 v17, v17\n" \
  "v_smfmac_f32_32x32x32_bf16 v[172:187], v[88:91], v[8:15], v236\n" \
  "s_setprio 0\n" \
  "v_exp_f32 v18, v18\n" \
  "v_exp_f32 v19, v19\n" \
  "v_exp_f32 v20, v20\n" \
  "v_exp_f32 v21, v21\n" \
  "v_exp_f32 v22, v22\n" \
  "v_exp_f32 v23, v23\n" \
  "v_exp_f32 v24, v24\n" \
  "v_exp_f32 v25, v25\n" \
  "v_smfmac_f32_32x32x32_bf16 v[172:187], v[92:95], v[16:23], v236\n" \
  "s_setprio 2\n" \
  "v_exp_f32 v26, v26\n" \
  "v_exp_f32 v27, v27\n" \
  "v_exp_f32 v28, v28\n" \
  "v_mfma_f32_32x32x16_bf16 v[8:23], v[84:87], v[64:67], 0\n" \
  "ds_read_b128 v[96:99], %5 offset:2048\n" \
  "v_exp_f32 v29, v29\n" \
  "v_exp_f32 v30, v30\n" \
  "v_exp_f32 v31, v31\n" \
  "v_exp_f32 v32, v32\n" \
  "v_exp_f32 v33, v33\n" \
  "v_smfmac_f32_32x32x32_bf16 v[188:203], v[88:91], v[24:31], v236\n" \
  "s_setprio 0\n" \
  "s_mov_b64 exec, s[60:61]\n" \
  "ds_read_b128 v[100:103], %6 offset:3072\n" \
  "ds_read_b128 v[104:107], %6 offset:3584\n" \
  "s_mov_b64 exec, -1\n" \
  "v_exp_f32 v34, v34\n" \
  "v_exp_f32 v35, v35\n" \
  "v_exp_f32 v36, v36\n" \
  "v_exp_f32 v37, v37\n" \
  "v_exp_f32 v38, v38\n" \
  "v_exp_f32 v39, v39\n" \
  "v_exp_f32 v8, v8\n" \
  "v_exp_f32 v9, v9\n" \
  "v_smfmac_f32_32x32x32_bf16 v[188:203], v[92:95], v[32:39], v236\n" \
  "s_setprio 2\n" \
  "v_exp_f32 v10, v10\n" \
  "v_exp_f32 v11, v11\n" \
  "v_exp_f32 v12, v12\n" \
  "v_mfma_f32_32x32x16_bf16 v[24:39], v[84:87], v[68:71], 0\n" \
  "s_nop 0\n" \
  "v_exp_f32 v13, v13\n" \
  "v_exp_f32 v14, v14\n" \
  "v_exp_f32 v15, v15\n" \
  "v_exp_f32 v16, v16\n" \
  "v_exp_f32 v17, v17\n" \
  "v_smfmac_f32_32x32x32_bf16 v[204:219], v[88:91], v[8:15], v236\n" \
  "s_setprio 0\n" \
  "v_exp_f32 v18, v18\n" \
  "v_exp_f32 v19, v19\n" \
  "v_exp_f32 v20, v20\n" \
  "v_exp_f32 v21, v21\n" \
  "v_exp_f32 v22, v22\n" \
  "v_exp_f32 v23, v23\n" \
  "v_exp_f32 v24, v24\n" \
  "v_exp_f32 v25, v25\n" \
  "v_smfmac_f32_32x32x32_bf16 v[204:219], v[92:95], v[16:23], v236\n" \
  "s_setprio 2\n" \
  "s_waitcnt lgkmcnt(0)\n" \
  "v_exp_f32 v26, v26\n" \
  "v_exp_f32 v27, v27\n" \
  "v_exp_f32 v28, v28\n" \
  "v_mfma_f32_32x32x16_bf16 v[8:23], v[96:99], v[40:43], 0\n" \
  "s_nop 0\n" \
  "v_exp_f32 v29, v29\n" \
  "v_exp_f32 v30, v30\n" \
  "v_exp_f32 v31, v31\n" \
  "v_exp_f32 v32, v32\n" \
  "v_exp_f32 v33, v33\n" \
  "v_smfmac_f32_32x32x32_bf16 v[220:235], v[88:91], v[24:31], v236\n" \
  "s_setprio 0\n" \
  "s_cbranch_vccz 40f\n" \
  "buffer_load_dwordx4 %7, %9, %2 offen offset:0 sc1 lds\n" \
  "buffer_load_dwordx4 %7, %9, %2 offen offset:1024 sc1 lds\n" \
  "buffer_load_dwordx4 %7, %9, %2 offen offset:2048 sc1 lds\n" \
  "s_add_u32 %2, %2, 3072\n" \
  "40:\n" \
  "v_exp_f32 v34, v34\n" \
  "v_exp_f32 v35, v35\n" \
  "v_exp_f32 v36, v36\n" \
  "v_exp_f32 v37, v37\n" \
  "v_exp_f32 v38, v38\n" \
  "v_exp_f32 v39, v39\n" \
    \
  "v_exp_f32 v8, v8\n" \
  "v_exp_f32 v9, v9\n" \
  "v_smfmac_f32_32x32x32_bf16 v[220:235], v[92:95], v[32:39], v236\n" \
  "s_setprio 2\n" \
  "s_add_u32 m0, %4, 3072\n" \
  "v_exp_f32 v10, v10\n" \
  "v_exp_f32 v11, v11\n" \
  "v_exp_f32 v12, v12\n" \
  "v_mfma_f32_32x32x16_bf16 v[24:39], v[96:99], v[44:47], 0\n" \
  "s_nop 0\n" \
  "v_exp_f32 v13, v13\n" \
  "v_exp_f32 v14, v14\n" \
  "v_exp_f32 v15, v15\n" \
  "v_exp_f32 v16, v16\n" \
  "v_exp_f32 v17, v17\n" \
  "v_smfmac_f32_32x32x32_bf16 v[108:123], v[100:103], v[8:15], v236\n" \
  "s_setprio 0\n" \
  "v_exp_f32 v18, v18\n" \
  "v_exp_f32 v19, v19\n" \
  "v_exp_f32 v20, v20\n" \
  "v_exp_f32 v21, v21\n" \
  "v_exp_f32 v22, v22\n" \
  "v_exp_f32 v23, v23\n" \
  "v_exp_f32 v24, v24\n" \
  "v_exp_f32 v25, v25\n" \
  "v_smfmac_f32_32x32x32_bf16 v[108:123], v[104:107], v[16:23], v236\n" \
  "s_setprio 2\n" \
  "v_exp_f32 v26, v26\n" \
  "v_exp_f32 v27, v27\n" \
  "v_exp_f32 v28, v28\n" \
  "v_mfma_f32_32x32x16_bf16 v[8:23], v[96:99], v[48:51], 0\n" \
  "s_nop 0\n" \
  "v_exp_f32 v29, v29\n" \
  "v_exp_f32 v30, v30\n" \
  "v_exp_f32 v31, v31\n" \
  "v_exp_f32 v32, v32\n" \
  "v_exp_f32 v33, v33\n" \
  "v_smfmac_f32_32x32x32_bf16 v[124:139], v[100:103], v[24:31], v236\n" \
  "s_setprio 0\n" \
  "s_cbranch_vccz 41f\n" \
  "buffer_load_dwordx4 %7, %9, %2 offen offset:0 sc1 lds\n" \
  "buffer_load_dwordx4 %7, %9, %2 offen offset:1024 sc1 lds\n" \
  "buffer_load_dwordx4 %7, %9, %2 offen offset:2048 sc1 lds\n" \
  "s_add_u32 %2, %2, 3072\n" \
  "41:\n" \
  "v_exp_f32 v34, v34\n" \
  "v_exp_f32 v35, v35\n" \
  "v_exp_f32 v36, v36\n" \
  "v_exp_f32 v37, v37\n" \
  "v_exp_f32 v38, v38\n" \
  "v_exp_f32 v39, v39\n" \
  "v_exp_f32 v8, v8\n" \
  "v_exp_f32 v9, v9\n" \
  "v_smfmac_f32_32x32x32_bf16 v[124:139], v[104:107], v[32:39], v236\n" \
  "s_setprio 2\n" \
  "v_exp_f32 v10, v10\n" \
  "v_exp_f32 v11, v11\n" \
  "v_exp_f32 v12, v12\n" \
  "v_mfma_f32_32x32x16_bf16 v[24:39], v[96:99], v[52:55], 0\n" \
  "s_nop 0\n" \
  "v_exp_f32 v13, v13\n" \
  "v_exp_f32 v14, v14\n" \
  "v_exp_f32 v15, v15\n" \
  "v_exp_f32 v16, v16\n" \
  "v_exp_f32 v17, v17\n" \
  "v_smfmac_f32_32x32x32_bf16 v[140:155], v[100:103], v[8:15], v236\n" \
  "s_setprio 0\n" \
  "v_exp_f32 v18, v18\n" \
  "v_exp_f32 v19, v19\n" \
  "v_exp_f32 v20, v20\n" \
  "v_exp_f32 v21, v21\n" \
  "v_exp_f32 v22, v22\n" \
  "v_exp_f32 v23, v23\n" \
  "v_exp_f32 v24, v24\n" \
  "v_exp_f32 v25, v25\n" \
  "v_smfmac_f32_32x32x32_bf16 v[140:155], v[104:107], v[16:23], v236\n" \
  "s_setprio 2\n" \
  "s_xor_b32 %4, %4, %16\n" \
  "v_exp_f32 v26, v26\n" \
  "v_exp_f32 v27, v27\n" \
  "v_exp_f32 v28, v28\n" \
  "v_mfma_f32_32x32x16_bf16 v[8:23], v[96:99], v[56:59], 0\n" \
  "s_cmp_eq_i32 %3, -1\n" \
  "v_exp_f32 v29, v29\n" \
  "v_exp_f32 v30, v30\n" \
  "v_exp_f32 v31, v31\n" \
  "v_exp_f32 v32, v32\n" \
  "v_exp_f32 v33, v33\n" \
  "v_smfmac_f32_32x32x32_bf16 v[156:171], v[100:103], v[24:31], v236\n" \
  "s_setprio 0\n" \
  "v_exp_f32 v34, v34\n" \
  "v_exp_f32 v35, v35\n" \
  "v_exp_f32 v36, v36\n" \
  "v_exp_f32 v37, v37\n" \
  "v_exp_f32 v38, v38\n" \
  "v_exp_f32 v39, v39\n" \
  "v_exp_f32 v8, v8\n" \
  "v_exp_f32 v9, v9\n" \
  "v_smfmac_f32_32x32x32_bf16 v[156:171], v[104:107], v[32:39], v236\n" \
  "s_setprio 2\n" \
  "v_exp_f32 v10, v10\n" \
  "v_exp_f32 v11, v11\n" \
  "v_exp_f32 v12, v12\n" \
  "v_mfma_f32_32x32x16_bf16 v[24:39], v[96:99], v[60:63], 0\n" \
  "s_nop 0\n" \
  "v_exp_f32 v13, v13\n" \
  "v_exp_f32 v14, v14\n" \
  "v_exp_f32 v15, v15\n" \
  "v_exp_f32 v16, v16\n" \
  "v_exp_f32 v17, v17\n" \
  "v_smfmac_f32_32x32x32_bf16 v[172:187], v[100:103], v[8:15], v236\n" \
  "s_setprio 0\n" \
  "v_exp_f32 v18, v18\n" \
  "v_exp_f32 v19, v19\n" \
  "v_exp_f32 v20, v20\n" \
  "v_exp_f32 v21, v21\n" \
  "v_exp_f32 v22, v22\n" \
  "v_exp_f32 v23, v23\n" \
  "v_exp_f32 v24, v24\n" \
  "v_exp_f32 v25, v25\n" \
  "v_smfmac_f32_32x32x32_bf16 v[172:187], v[104:107], v[16:23], v236\n" \
  "s_setprio 2\n" \
  "v_exp_f32 v26, v26\n" \
  "v_exp_f32 v27, v27\n" \
  "v_exp_f32 v28, v28\n" \
  "v_mfma_f32_32x32x16_bf16 v[8:23], v[96:99], v[64:67], 0\n" \
  "ds_read_b128 v[72:75], %5 offset:4096\n" \
  "v_exp_f32 v29, v29\n" \
  "v_exp_f32 v30, v30\n" \
  "v_exp_f32 v31, v31\n" \
  "v_exp_f32 v32, v32\n" \
  "v_exp_f32 v33, v33\n" \
  "v_smfmac_f32_32x32x32_bf16 v[188:203], v[100:103], v[24:31], v236\n" \
  "s_setprio 0\n" \
  "s_mov_b64 exec, s[60:61]\n" \
  "ds_read_b128 v[76:79], %6 offset:5120\n" \
  "ds_read_b128 v[80:83], %6 offset:5632\n" \
  "s_mov_b64 exec, -1\n" \
  "v_exp_f32 v34, v34\n" \
  "v_exp_f32 v35, v35\n" \
  "v_exp_f32 v36, v36\n" \
  "v_exp_f32 v37, v37\n" \
  "v_exp_f32 v38, v38\n" \
  "v_exp_f32 v39, v39\n" \
  "v_exp_f32 v8, v8\n" \
  "v_exp_f32 v9, v9\n" \
  "v_smfmac_f32_32x32x32_bf16 v[188:203], v[104:107], v[32:39], v236\n" \
  "s_setprio 2\n" \
  "v_exp_f32 v10, v10\n" \
  "v_exp_f32 v11, v11\n" \
  "v_exp_f32 v12, v12\n" \
  "v_mfma_f32_32x32x16_bf16 v[24:39], v[96:99], v[68:71], 0\n" \
  "s_nop 0\n" \
  "v_exp_f32 v13, v13\n" \
  "v_exp_f32 v14, v14\n" \
  "v_exp_f32 v15, v15\n" \
  "v_exp_f32 v16, v16\n" \
  "v_exp_f32 v17, v17\n" \
  "v_smfmac_f32_32x32x32_bf16 v[204:219], v[100:103], v[8:15], v236\n" \
  "s_setprio 0\n" \
  "v_exp_f32 v18, v18\n" \
  "v_exp_f32 v19, v19\n" \
  "v_exp_f32 v20, v20\n" \
  "v_exp_f32 v21, v21\n" \
  "v_exp_f32 v22, v22\n" \
  "v_exp_f32 v23, v23\n" \
  "v_exp_f32 v24, v24\n" \
  "v_exp_f32 v25, v25\n" \
  "v_smfmac_f32_32x32x32_bf16 v[204:219], v[104:107], v[16:23], v236\n" \
  "s_setprio 2\n" \
  "s_waitcnt lgkmcnt(0)\n" \
  "v_exp_f32 v26, v26\n" \
  "v_exp_f32 v27, v27\n" \
  "v_exp_f32 v28, v28\n" \
  "v_mfma_f32_32x32x16_bf16 v[8:23], v[72:75], v[40:43], 0\n" \
  "s_nop 0\n" \
  "v_exp_f32 v29, v29\n" \
  "v_exp_f32 v30, v30\n" \
  "v_exp_f32 v31, v31\n" \
  "v_exp_f32 v32, v32\n" \
  "v_exp_f32 v33, v33\n" \
  "v_smfmac_f32_32x32x32_bf16 v[220:235], v[100:103], v[24:31], v236\n" \
  "s_setprio 0\n" \
  "v_xor_b32 %5, %16, %5\n" \
  "v_xor_b32 %6, %16, %6\n" \
  "s_cbranch_scc0 1b\n" \
  "v_exp_f32 v34, v34\n" \
  "v_exp_f32 v35, v35\n" \
  "v_exp_f32 v36, v36\n" \
  "v_exp_f32 v37, v37\n" \
  "v_exp_f32 v38, v38\n" \
  "v_exp_f32 v39, v39\n" \
  "v_xor_b32 %5, %16, %5\n" \
    \
  "v_exp_f32 v8, v8\n" \
  "v_exp_f32 v9, v9\n" \
  "v_smfmac_f32_32x32x32_bf16 v[220:235], v[104:107], v[32:39], v236\n" \
  "s_setprio 2\n" \
  "ds_read_b32 v40, %10 offset:8\n" \
  "v_exp_f32 v10, v10\n" \
  "v_exp_f32 v11, v11\n" \
  "v_exp_f32 v12, v12\n" \
  "v_mfma_f32_32x32x16_bf16 v[24:39], v[72:75], v[44:47], 0\n" \
  "s_nop 0\n" \
  "v_exp_f32 v13, v13\n" \
  "v_exp_f32 v14, v14\n" \
  "v_exp_f32 v15, v15\n" \
  "v_exp_f32 v16, v16\n" \
  "v_exp_f32 v17, v17\n" \
  "v_smfmac_f32_32x32x32_bf16 v[108:123], v[76:79], v[8:15], v236\n" \
  "s_setprio 0\n" \
  "v_exp_f32 v18, v18\n" \
  "v_exp_f32 v19, v19\n" \
  "v_exp_f32 v20, v20\n" \
  "v_exp_f32 v21, v21\n" \
  "v_exp_f32 v22, v22\n" \
  "v_exp_f32 v23, v23\n" \
  "v_exp_f32 v24, v24\n" \
  "v_exp_f32 v25, v25\n" \
  "v_smfmac_f32_32x32x32_bf16 v[108:123], v[80:83], v[16:23], v236\n" \
  "s_setprio 2\n" \
  "v_exp_f32 v26, v26\n" \
  "v_exp_f32 v27, v27\n" \
  "v_exp_f32 v28, v28\n" \
  "v_mfma_f32_32x32x16_bf16 v[8:23], v[72:75], v[48:51], 0\n" \
  "s_nop 0\n" \
  "v_exp_f32 v29, v29\n" \
  "v_exp_f32 v30, v30\n" \
  "v_exp_f32 v31, v31\n" \
  "v_exp_f32 v32, v32\n" \
  "v_exp_f32 v33, v33\n" \
  "v_smfmac_f32_32x32x32_bf16 v[124:139], v[76:79], v[24:31], v236\n" \
  "s_setprio 0\n" \
  "s_waitcnt lgkmcnt(0)\n" \
  "v_cmp_gt_u32 vcc, 4, v40\n" \
  "s_cbranch_vccnz 54f\n" \
  "55:\n" \
  "v_exp_f32 v34, v34\n" \
  "v_exp_f32 v35, v35\n" \
  "v_exp_f32 v36, v36\n" \
  "v_exp_f32 v37, v37\n" \
  "v_exp_f32 v38, v38\n" \
  "v_exp_f32 v39, v39\n" \
  "v_exp_f32 v8, v8\n" \
  "v_exp_f32 v9, v9\n" \
  "v_smfmac_f32_32x32x32_bf16 v[124:139], v[80:83], v[32:39], v236\n" \
  "s_setprio 2\n" \
  "ds_write_b128 %5, v[108:111] offset:0\n" \
  "v_exp_f32 v10, v10\n" \
  "v_exp_f32 v11, v11\n" \
  "v_exp_f32 v12, v12\n" \
  "v_mfma_f32_32x32x16_bf16 v[24:39], v[72:75], v[52:55], 0\n" \
  "s_nop 0\n" \
  "v_exp_f32 v13, v13\n" \
  "v_exp_f32 v14, v14\n" \
  "v_exp_f32 v15, v15\n" \
  "v_exp_f32 v16, v16\n" \
  "v_exp_f32 v17, v17\n" \
  "v_smfmac_f32_32x32x32_bf16 v[140:155], v[76:79], v[8:15], v236\n" \
  "s_setprio 0\n" \
  "ds_write_b128 %5, v[112:115] offset:1024\n" \
  "v_exp_f32 v18, v18\n" \
  "v_exp_f32 v19, v19\n" \
  "v_exp_f32 v20, v20\n" \
  "v_exp_f32 v21, v21\n" \
  "v_exp_f32 v22, v22\n" \
  "v_exp_f32 v23, v23\n" \
  "v_exp_f32 v24, v24\n" \
  "v_exp_f32 v25, v25\n" \
  "v_smfmac_f32_32x32x32_bf16 v[140:155], v[80:83], v[16:23], v236\n" \
  "s_setprio 2\n" \
  "ds_write_b128 %5, v[124:127] offset:2048\n" \
  "v_exp_f32 v26, v26\n" \
  "v_exp_f32 v27, v27\n" \
  "v_exp_f32 v28, v28\n" \
  "v_mfma_f32_32x32x16_bf16 v[8:23], v[72:75], v[56:59], 0\n" \
  "ds_write2_b32 %13, v116, v132 offset0:0 offset1:1\n" \
  "v_exp_f32 v29, v29\n" \
  "v_exp_f32 v30, v30\n" \
  "v_exp_f32 v31, v31\n" \
  "v_exp_f32 v32, v32\n" \
  "v_exp_f32 v33, v33\n" \
  "v_smfmac_f32_32x32x32_bf16 v[156:171], v[76:79], v[24:31], v236\n" \
  "s_setprio 0\n" \
  "ds_write_b128 %5, v[128:131] offset:3072\n" \
  "v_exp_f32 v34, v34\n" \
  "v_exp_f32 v35, v35\n" \
  "v_exp_f32 v36, v36\n" \
  "v_exp_f32 v37, v37\n" \
  "v_exp_f32 v38, v38\n" \
  "v_exp_f32 v39, v39\n" \
  "v_exp_f32 v8, v8\n" \
  "v_exp_f32 v9, v9\n" \
  "v_smfmac_f32_32x32x32_bf16 v[156:171], v[80:83], v[32:39], v236\n" \
  "s_setprio 2\n" \
  "ds_write_b128 %5, v[140:143] offset:4096\n" \
  "v_exp_f32 v10, v10\n" \
  "v_exp_f32 v11, v11\n" \
  "v_exp_f32 v12, v12\n" \
  "v_mfma_f32_32x32x16_bf16 v[24:39], v[72:75], v[60:63], 0\n" \
  "s_nop 0\n" \
  "v_exp_f32 v13, v13\n" \
  "v_exp_f32 v14, v14\n" \
  "v_exp_f32 v15, v15\n" \
  "v_exp_f32 v16, v16\n" \
  "v_exp_f32 v17, v17\n" \
  "v_smfmac_f32_32x32x32_bf16 v[172:187], v[76:79], v[8:15], v236\n" \
  "s_setprio 0\n" \
  "ds_write_b128 %5, v[144:147] offset:5120\n" \
  "v_exp_f32 v18, v18\n" \
  "v_exp_f32 v19, v19\n" \
  "v_exp_f32 v20, v20\n" \
  "v_exp_f32 v21, v21\n" \
  "v_exp_f32 v22, v22\n" \
  "v_exp_f32 v23, v23\n" \
  "v_exp_f32 v24, v24\n" \
  "v_exp_f32 v25, v25\n" \
  "v_smfmac_f32_32x32x32_bf16 v[172:187], v[80:83], v[16:23], v236\n" \
  "s_setprio 2\n" \
  "ds_write_b128 %5, v[156:159] offset:6144\n" \
  "v_exp_f32 v26, v26\n" \
  "v_exp_f32 v27, v27\n" \
  "v_exp_f32 v28, v28\n" \
  "v_mfma_f32_32x32x16_bf16 v[8:23], v[72:75], v[64:67], 0\n" \
  "ds_write2_b32 %13, v148, v164 offset0:2 offset1:3\n" \
  "v_exp_f32 v29, v29\n" \
  "v_exp_f32 v30, v30\n" \
  "v_exp_f32 v31, v31\n" \
  "v_exp_f32 v32, v32\n" \
  "v_exp_f32 v33, v33\n" \
  "v_smfmac_f32_32x32x32_bf16 v[188:203], v[76:79], v[24:31], v236\n" \
  "s_setprio 0\n" \
  "ds_write_b128 %5, v[160:163] offset:7168\n" \
  "v_exp_f32 v34, v34\n" \
  "v_exp_f32 v35, v35\n" \
  "v_exp_f32 v36, v36\n" \
  "v_exp_f32 v37, v37\n" \
  "v_exp_f32 v38, v38\n" \
  "v_exp_f32 v39, v39\n" \
  "v_exp_f32 v8, v8\n" \
  "v_exp_f32 v9, v9\n" \
  "v_smfmac_f32_32x32x32_bf16 v[188:203], v[80:83], v[32:39], v236\n" \
  "s_setprio 2\n" \
  "ds_write_b128 %5, v[172:175] offset:8192\n" \
  "v_exp_f32 v10, v10\n" \
  "v_exp_f32 v11, v11\n" \
  "v_exp_f32 v12, v12\n" \
  "v_mfma_f32_32x32x16_bf16 v[24:39], v[72:75], v[68:71], 0\n" \
  "s_nop 0\n" \
  "v_exp_f32 v13, v13\n" \
  "v_exp_f32 v14, v14\n" \
  "v_exp_f32 v15, v15\n" \
  "v_exp_f32 v16, v16\n" \
  "v_exp_f32 v17, v17\n" \
  "v_smfmac_f32_32x32x32_bf16 v[204:219], v[76:79], v[8:15], v236\n" \
  "s_setprio 0\n" \
  "ds_write_b128 %5, v[176:179] offset:9216\n" \
  "v_exp_f32 v18, v18\n" \
  "v_exp_f32 v19, v19\n" \
  "v_exp_f32 v20, v20\n" \
  "v_exp_f32 v21, v21\n" \
  "v_exp_f32 v22, v22\n" \
  "v_exp_f32 v23, v23\n" \
  "ds_write_b128 %5, v[188:191] offset:10240\n" \
  "ds_write_b128 %5, v[192:195] offset:11264\n" \
  "ds_write2_b32 %13, v180, v196 offset0:4 offset1:5\n" \
  "v_exp_f32 v24, v24\n" \
  "v_exp_f32 v25, v25\n" \
  "v_smfmac_f32_32x32x32_bf16 v[204:219], v[80:83], v[16:23], v236\n" \
  "s_setprio 2\n" \
  "s_mov_b64 exec, 1\n" \
  "ds_add_u32 %10, %11 offset:0\n" \
  "s_mov_b64 exec, -1\n" \
  "v_exp_f32 v26, v26\n" \
  "v_exp_f32 v27, v27\n" \
  "v_exp_f32 v28, v28\n" \
  "v_exp_f32 v29, v29\n" \
  "v_exp_f32 v30, v30\n" \
  "v_exp_f32 v31, v31\n" \
  "v_exp_f32 v32, v32\n" \
  "v_exp_f32 v33, v33\n" \
  "v_smfmac_f32_32x32x32_bf16 v[220:235], v[76:79], v[24:31], v236\n" \
  "s_setprio 0\n" \
  "v_exp_f32 v34, v34\n" \
  "v_exp_f32 v35, v35\n" \
  "v_exp_f32 v36, v36\n" \
  "v_exp_f32 v37, v37\n" \
  "v_exp_f32 v38, v38\n" \
  "v_exp_f32 v39, v39\n" \
  "ds_read_b32 v40, %10 offset:0\n" \
  "s_setprio 3\n" \
  "v_smfmac_f32_32x32x32_bf16 v[220:235], v[80:83], v[32:39], v236\n" \
  "v_xor_b32 v41, 1024, %13\n" \
  "v_xor_b32 v42, 2048, %13\n" \
  "v_xor_b32 v43, 3072, %13\n" \
  "s_mov_b32 %3, 0x400000\n" \
  "s_branch 31f\n" \
  "30:\n" \
  "s_sub_u32 %3, %3, 1\n" \
  "s_cbranch_scc1 32f\n" \
  "ds_read_b32 v40, %10 offset:0\n" \
  "31:\n" \
  "s_waitcnt lgkmcnt(0)\n" \
  "v_cmp_gt_u32 vcc, 9, v40\n" \
  "s_cbranch_vccnz 30b\n" \
  "32:\n" \
    \
  "ds_read_b64 v[156:157], v41 offset:0\n" \
  "ds_read_b64 v[158:159], v42 offset:8\n" \
  "ds_read_b64 v[160:161], v43 offset:16\n" \
  "ds_read_b128 v[108:111], %17 offset:0\n" \
  "ds_read_b128 v[124:127], %18 offset:0\n" \
  "ds_read_b128 v[140:143], %19 offset:0\n" \
  "ds_read_b128 v[112:115], %17 offset:1024\n" \
  "ds_read_b128 v[128:131], %18 offset:1024\n" \
  "ds_read_b128 v[144:147], %19 offset:1024\n" \
  "ds_read_b128 v[116:119], %17 offset:2048\n" \
  "ds_read_b128 v[132:135], %18 offset:2048\n" \
  "ds_read_b128 v[148:151], %19 offset:2048\n" \
  "ds_read_b128 v[120:123], %17 offset:3072\n" \
  "ds_read_b128 v[136:139], %18 offset:3072\n" \
  "ds_read_b128 v[152:155], %19 offset:3072\n" \
  "v_mov_b32 v213, v228\n" \
  "s_waitcnt lgkmcnt(12)\n" \
  ".long 0xd3b2409c, 0x1803a99c\n" \
  ".long 0xd3b2409e, 0x1803419e\n" \
  "s_waitcnt lgkmcnt(9)\n" \
  ".long 0xd3b240cc, 0x1802d9cc\n" \
  ".long 0xd3b2409c, 0x18033d9c\n" \
  ".long 0xd3b2407c, 0x1803197c\n" \
  ".long 0xd3b240ce, 0x1802ddce\n" \
  ".long 0xd3b2407e, 0x18031d7e\n" \
  "v_rcp_f32 v158, v156\n" \
  "v_rcp_f32 v160, v157\n" \
  "s_waitcnt lgkmcnt(6)\n" \
  ".long 0xd3b240d0, 0x1802e1d0\n" \
  ".long 0xd3b24080, 0x18032180\n" \
  ".long 0xd3b240d2, 0x1802e5d2\n" \
  ".long 0xd3b24082, 0x18032582\n" \
  "s_waitcnt lgkmcnt(3)\n" \
  ".long 0xd3b240dc, 0x1802e9dc\n" \
  ".long 0xd3b24084, 0x18032984\n" \
  ".long 0xd3b240de, 0x1802edde\n" \
  ".long 0xd3b24086, 0x18032d86\n" \
  "s_waitcnt lgkmcnt(0)\n" \
  ".long 0xd3b240e0, 0x1802f1e0\n" \
  ".long 0xd3b24088, 0x18033188\n" \
  ".long 0xd3b240e2, 0x1802f5e2\n" \
  ".long 0xd3b2408a, 0x1803358a\n" \
  "v_min_f32 v162, v156, v157\n" \
  "v_max_f32 v163, v156, v157\n" \
  ".long 0xd3b240cc, 0x1802f9cc\n" \
  ".long 0xd3b240ce, 0x1802fdce\n" \
  ".long 0xd3b240d0, 0x180301d0\n" \
  ".long 0xd3b240d2, 0x180305d2\n" \
  ".long 0xd3b240dc, 0x180309dc\n" \
  ".long 0xd3b240de, 0x18030dde\n" \
  ".long 0xd3b240e0, 0x180311e0\n" \
  ".long 0xd3b240e2, 0x180315e2\n" \
  ".long 0xd3b140cc, 0x08033dcc\n" \
  ".long 0xd3b140ce, 0x08033dce\n" \
  ".long 0xd3b140d0, 0x08033dd0\n" \
  ".long 0xd3b140d2, 0x08033dd2\n" \
  ".long 0xd3b140dc, 0x080341dc\n" \
  ".long 0xd3b140de, 0x080341de\n" \
  ".long 0xd3b140e0, 0x080341e0\n" \
  ".long 0xd3b140e2, 0x080341e2\n" \
  ".long 0xd3b2406c, 0x1803b9cc\n" \
  ".long 0xd3b2406e, 0x1803bdce\n" \
  ".long 0xd3b24070, 0x1803c1d0\n" \
  ".long 0xd3b24072, 0x1803c5d2\n" \
  "v_cvt_pk_bf16_f32 v216, v204, v205\n" \
  "v_cvt_pk_bf16_f32 v217, v206, v207\n" \
  "v_cvt_pk_bf16_f32 v218, v208, v209\n" \
  "v_cvt_pk_bf16_f32 v219, v210, v211\n" \
  ".long 0xd3b2406c, 0x1802dd6c\n" \
  ".long 0xd3b24070, 0x1802e570\n" \
  "v_cvt_pk_bf16_f32 v232, v220, v221\n" \
  "v_cvt_pk_bf16_f32 v233, v222, v223\n" \
  "v_cvt_pk_bf16_f32 v234, v224, v225\n" \
  "v_cvt_pk_bf16_f32 v235, v226, v227\n" \
  ".long 0xd3b2406c, 0x1802e16c\n" \
  "v_cmp_nle_f32 vcc, 0x21800000, v162\n" \
  "v_add_f32 v108, v108, v109\n" \
  "s_or_b32 %3, vcc_lo, vcc_hi\n" \
  "v_cmp_nge_f32 vcc, 0x67800000, v163\n" \
  "v_mul_f32 v108, 0, v108\n" \
  "s_or_b32 %3, %3, vcc_lo\n" \
  "s_or_b32 %3, %3, vcc_hi\n" \
  "v_cmp_u_f32 vcc, v108, v108\n" \
  "s_or_b32 %3, %3, vcc_lo\n" \
  "s_or_b32 %3, %3, vcc_hi\n" \
  "s_branch 52f\n" \
    \
  "50:\n" \
  "s_mov_b32 m0, 0x400000\n" \
  "53:\n" \
  "ds_read_b32 v84, %14 offset:16\n" \
  "s_sub_u32 m0, m0, 1\n" \
  "s_cbranch_scc1 51b\n" \
  "s_waitcnt lgkmcnt(0)\n" \
  "v_cmp_gt_u32 vcc, 2, v84\n" \
  "s_cbranch_vccnz 53b\n" \
  "s_branch 51b\n" \
  "54:\n" \
  "s_mov_b32 m0, 0x400000\n" \
  "56:\n" \
  "ds_read_b32 v40, %10 offset:8\n" \
  "s_sub_u32 m0, m0, 1\n" \
  "s_cbranch_scc1 55b\n" \
  "s_waitcnt lgkmcnt(0)\n" \
  "v_cmp_gt_u32 vcc, 4, v40\n" \
  "s_cbranch_vccnz 56b\n" \
  "s_branch 55b\n" \
  "74:\n" \
  "s_mov_b32 m0, 0x400000\n" \
  "76:\n" \
  "ds_read_b32 v29, %10 offset:68\n" \
  "s_sub_u32 m0, m0, 1\n" \
  "s_cbranch_scc1 75b\n" \
  "s_waitcnt lgkmcnt(0)\n" \
  "v_cmp_gt_u32 vcc, 3, v29\n" \
  "s_cbranch_vccnz 76b\n" \
  "s_branch 75b\n" \
  "52:\n"
#define ATT_CLOB "v8","v9","v10","v11","v12","v13","v14","v15","v16","v17","v18","v19","v20","v21","v22","v23","v24","v25","v26","v27","v28","v29","v30","v31","v32","v33","v34","v35","v36","v37","v38","v39","v40","v41","v42","v43","v44","v45","v46","v47","v48","v49","v50","v51","v52","v53","v54","v55","v56","v57","v58","v59","v60","v61","v62","v63","v64","v65","v66","v67","v68","v69","v70","v71","v72","v73","v74","v75","v76","v77","v78","v79","v80","v81","v82","v83","v84","v85","v86","v87","v88","v89","v90","v91","v92","v93","v94","v95","v96","v97","v98","v99","v100","v101","v102","v103","v104","v105","v106","v107","v108","v109","v110","v111","v112","v113","v114","v115","v116","v117","v118","v119","v120","v121","v122","v123","v124","v125","v126","v127","v128","v129","v130","v131","v132","v133","v134","v135","v136","v137","v138","v139","v140","v141","v142","v143","v144","v145","v146","v147","v148","v149","v150","v151","v152","v153","v154","v155","v156","v157","v158","v159","v160","v161","v162","v163","v164","v165","v166","v167","v168","v169","v170","v171","v172","v173","v174","v175","v176","v177","v178","v179","v180","v181","v182","v183","v184","v185","v186","v187","v188","v189","v190","v191","v192","v193","v194","v195","v196","v197","v198","v199","v200","v201","v202","v203","v204","v205","v206","v207","v208","v209","v210","v211","v212","v213","v214","v215","v220","v221","v222","v223","v224","v225","v226","v227","v228","v229","v230","v231","v236","s60","s61"

constexpr unsigned FS = 128;
constexpr unsigned TS = 2048;
constexpr unsigned PREC = 2176;
constexpr unsigned WS_GEN = 0;
constexpr unsigned WS_FKV = 4096;
constexpr unsigned WS_FC = WS_FKV + 256 * FS;
constexpr unsigned WS_KV = WS_FC + 2048 * FS;
constexpr unsigned WS_R = WS_KV + 64 * TS;
constexpr unsigned WS_P = WS_R + 32 * 4096;
constexpr unsigned WS_OA = WS_P + 1024 * PREC;
constexpr unsigned WS_OB = WS_OA + 2097152;
constexpr unsigned WS_END = WS_OB + 2097152;
constexpr unsigned BLK = 8320, STG = 4 * BLK;
constexpr unsigned L_WKV = 4 * STG;
constexpr unsigned L_KVR = 100352;
constexpr unsigned L_CNT = L_WKV + 16384, L_SCR = 151552, L_Q = 155648;
constexpr unsigned C_T = 0, C_G = 4, C_RF = 8, C_KVR = 16, C_TD = 32, C_S = 48, C_WB = 56, C_W = 64;
constexpr unsigned LDS_BYTES = 163840;
constexpr unsigned SPIN_CAP = 1u << 18;
constexpr unsigned NONE = 0x80000000u;

#if defined(__HIP_DEVICE_COMPILE__)
#pragma clang attribute push(__attribute__((target("no-packed-fp32-ops"))), apply_to = function)
#endif
#define MFMA32(a, b, c) __builtin_amdgcn_mfma_f32_32x32x16_bf16(a, b, c, 0, 0, 0)
#define MFMA16(a, b, c) __builtin_amdgcn_mfma_f32_16x16x32_bf16(a, b, c, 0, 0, 0)
#define VMCNT(n) asm volatile("s_waitcnt vmcnt(%0)" ::"n"(n) : "memory")
#define BARRIER() asm volatile("s_waitcnt lgkmcnt(0)\n\ts_barrier" ::: "memory")
#define SB() __builtin_amdgcn_sched_barrier(0)
template <class T> DEV void PIN(T& x) { asm volatile("" : "+v"(x)); }
#define IC(n) std::integral_constant<int, (n)>{}

DEV u32x4 mkdesc(const void* p, unsigned bytes) {
  unsigned long long a = (unsigned long long)p;
  return (u32x4){(unsigned)a, (unsigned)(a >> 32) & 0xffffu, bytes, 0x00020000u};
}
DEV rsrc_t mkrsrc(const void* p, unsigned bytes) { return __builtin_amdgcn_make_buffer_rsrc((void*)p, 0, bytes, 0x00020000); }
template <unsigned LI>
DEV void dma(u32x4 d, unsigned voff, unsigned& so, unsigned inc, unsigned lb) {
  asm volatile("s_add_u32 m0, %2, %5\n\ts_add_u32 %0, %0, %1\n\tbuffer_load_dwordx4 %3, %4, %0 offen lds"
               : "+s"(so) : "s"(inc), "s"(lb), "v"(voff), "s"(d), "n"(LI) : "scc", "memory");
}
template <unsigned LI> DEV void setm0(unsigned lb) { asm volatile("s_add_u32 m0, %0, %1\n\ts_nop 0" ::"s"(lb), "n"(LI) : "scc", "memory"); }
template <unsigned OFF> DEV void dmi(u32x4 d, unsigned voff, unsigned so) {
  asm volatile("buffer_load_dwordx4 %0, %1, %2 offen offset:%3 lds" ::"v"(voff), "s"(d), "s"(so), "n"(OFF) : "memory");
}
template <unsigned OFF> DEV void dmi_dev(u32x4 d, unsigned voff, unsigned so) {
  asm volatile("buffer_load_dwordx4 %0, %1, %2 offen offset:%3 sc1 lds" ::"v"(voff), "s"(d), "s"(so), "n"(OFF) : "memory");
}
template <int CNT>
DEV void dma_xb(u32x4 d, unsigned& voff, unsigned sb, unsigned lb, unsigned& n) {
  unsigned t, c;
  asm volatile("s_mov_b32 %1, %7\n1:\n\ts_and_b32 %2, %2, 7\n\ts_lshl_b32 %0, %2, 10\n\ts_add_u32 m0, %5, %0\n\ts_lshl_b32 %0, %2, 13\n\t"
               "s_add_u32 %0, %0, %4\n\tbuffer_load_dwordx4 %3, %6, %0 offen lds\n\tv_xor_b32 %3, 64, %3\n\ts_add_u32 %2, %2, 1\n\ts_sub_u32 %1, %1, 1\n\ts_cbranch_scc0 1b"
               : "=&s"(t), "=&s"(c), "+s"(n), "+v"(voff) : "s"(sb), "s"(lb), "s"(d), "n"(CNT - 1) : "scc", "memory");
}
DEV void touch(u32x4 d, unsigned voff, unsigned so) {
  asm volatile("s_mov_b32 m0, %0\n\ts_nop 0\n\tbuffer_load_dword %1, %2, %3 offen lds" ::"s"(L_CNT + 256), "v"(voff), "s"(d), "s"(so) : "memory");
}
DEV bf16x8 ldsr(unsigned a) { return *(const LAS bf16x8*)(size_t)a; }
DEV bf16x8 ldstr(unsigned a0, unsigned a1) {
  s16x4 lo = __builtin_amdgcn_ds_read_tr16_b64_v4i16((LAS s16x4*)(size_t)a0);
  s16x4 hi = __builtin_amdgcn_ds_read_tr16_b64_v4i16((LAS s16x4*)(size_t)a1);
  return __builtin_bit_cast(bf16x8, __builtin_shufflevector(lo, hi, 0, 1, 2, 3, 4, 5, 6, 7));
}
template <unsigned O>
DEV void lds_signal(unsigned cb, unsigned one) {
  asm volatile("s_waitcnt lgkmcnt(0)\n\ts_mov_b64 exec, 1\n\tds_add_u32 %0, %1 offset:%2\n\ts_mov_b64 exec, -1" ::"v"(cb), "v"(one), "n"(O) : "memory");
}
template <unsigned O, unsigned TARGET, int SLEEP>
DEV void lds_wait(unsigned cb, unsigned& bud) {
  unsigned t;
  asm volatile("s_branch 2f\n1:\n\ts_sleep %5\n\ts_sub_u32 %1, %1, 1\n\ts_cbranch_scc1 3f\n2:\n\tds_read_b32 %0, %2 offset:%3\n\ts_waitcnt lgkmcnt(0)\n\t"
               "v_cmp_gt_u32 vcc, %4, %0\n\ts_cbranch_vccnz 1b\n\ts_branch 4f\n3:\n\ts_mov_b32 %1, 0\n4:"
               : "=&v"(t), "+s"(bud) : "v"(cb), "n"(O), "n"(TARGET), "n"(SLEEP) : "vcc", "scc", "memory");
}
DEV unsigned fbits(float x) { return __builtin_bit_cast(unsigned, x); }
DEV float asf(unsigned x) { return __builtin_bit_cast(float, x); }
DEV bool wave_any(bool p) { return __builtin_amdgcn_ballot_w64(p) != 0; }
DEV unsigned pk2(float a, float b) { return __builtin_bit_cast(unsigned, __builtin_convertvector((f32x2){a, b}, bf16x2)); }
DEV bf16x8 asfrag(unsigned a, unsigned b, unsigned c, unsigned d) { return __builtin_bit_cast(bf16x8, (u32x4){a, b, c, d}); }
DEV bf16x8 asfrag(u32x4 v) { return __builtin_bit_cast(bf16x8, v); }
DEV float phi1(float t) {
  const float u = t * 1.44269504f;
  const float e = __builtin_amdgcn_fmed3f(__builtin_amdgcn_exp2f(u), 0.f, 1.f);
  return __builtin_fmaf(__builtin_fmaxf(u, 1.44269504e-6f), 0.693147181f, e);
}
DEV unsigned phi2(float a, float b) { return pk2(phi1(a), phi1(b)); }
DEV u32x4 ld128(rsrc_t ws, unsigned v, unsigned s) { return __builtin_amdgcn_raw_buffer_load_b128(ws, v, s, 16); }
DEV unsigned ld32(rsrc_t ws, unsigned v, unsigned s) { return __builtin_amdgcn_raw_buffer_load_b32(ws, v, s, 16); }
DEV void st128(u32x4 x, rsrc_t ws, unsigned v) { __builtin_amdgcn_raw_buffer_store_b128(x, ws, v, 0, 16); }
DEV void publish(rsrc_t ws, unsigned flag_off, unsigned epoch, int lane) {
  VMCNT(0);
  if (lane == 0) __builtin_amdgcn_raw_buffer_store_b32(epoch, ws, flag_off, 0, 16);
}
template <int SLEEP>
DEV void wait_flags(rsrc_t ws, unsigned vo, unsigned v, unsigned epoch) {
#pragma unroll 1
  for (unsigned s = 0; s < SPIN_CAP && wave_any(v != epoch); ++s) {
    __builtin_amdgcn_s_sleep(SLEEP);
    asm volatile("" ::: "memory");
    if (v != epoch) v = ld32(ws, vo, 0);
  }
  asm volatile("" ::: "memory");
}
DEV u32x4 norm_frag(const f32x16& o, float l) {
  float r = __builtin_amdgcn_rcpf(l);
  return (u32x4){pk2(o[0] * r, o[1] * r), pk2(o[2] * r, o[3] * r), pk2(o[4] * r, o[5] * r), pk2(o[6] * r, o[7] * r)};
}
DEV bf16x16 lo8(f32x16 s) { return __builtin_bit_cast(bf16x16, __builtin_shufflevector(s, s, 0, 1, 2, 3, 4, 5, 6, 7)); }
DEV bf16x16 hi8(f32x16 s) { return __builtin_bit_cast(bf16x16, __builtin_shufflevector(s, s, 8, 9, 10, 11, 12, 13, 14, 15)); }
DEV void swap16(float& a, float& c) { asm volatile("s_nop 1\n\tv_permlane16_swap_b32 %0, %1" : "+v"(a), "+v"(c)); }
DEV void swap32(float& a, float& c) { asm volatile("s_nop 1\n\tv_permlane32_swap_b32 %0, %1" : "+v"(a), "+v"(c)); }
template <int CTRL> DEV float dpp(float v) { return __builtin_bit_cast(float, __builtin_amdgcn_update_dpp(0, __builtin_bit_cast(int, v), CTRL, 0xf, 0xf, true)); }

DEV f32x16 attn_true_max(rsrc_t ws, bf16x8 q, unsigned vk, unsigned vv, unsigned cone) {
  float m = -__builtin_inff();
  const f32x16 zero = {};
#pragma unroll 1
  for (int t = 0; t < 64; ++t) {
    f32x16 s = MFMA32(asfrag(ld128(ws, vk, WS_KV + t * TS)), q, zero);
#pragma unroll
    for (int i = 0; i < 16; ++i) m = __builtin_fmaxf(m, s[i]);
  }
  float m2 = m;
  PIN(m2);
  swap32(m, m2);
  m = __builtin_fmaxf(m, m2);
  f32x16 o = {};
#pragma unroll 1
  for (int t = 0; t < 64; ++t) {
    f32x16 s = MFMA32(asfrag(ld128(ws, vk, WS_KV + t * TS)), q, zero);
#pragma unroll
    for (int i = 0; i < 16; ++i) s[i] = __builtin_amdgcn_exp2f(s[i] - m);
    o = __builtin_amdgcn_smfmac_f32_32x32x32_bf16(asfrag(ld128(ws, vv, WS_KV + t * TS + 1024) | cone), lo8(s), o, 0xDDDD, 0, 0);
    o = __builtin_amdgcn_smfmac_f32_32x32x32_bf16(asfrag(ld128(ws, vv, WS_KV + t * TS + 1536) | cone), hi8(s), o, 0xDDDD, 0, 0);
  }
  return o;
}
template <int N, class F> DEV void sfor(F&& f) {
  if constexpr (N > 0) { sfor<N - 1>(f); f(IC(N - 1)); }
}
DEV unsigned umax3(unsigned a, unsigned b, unsigned c) { unsigned m = a > b ? a : b; return m > c ? m : c; }
DEV bool cleared(const u32x4& a, const u32x4& b) {
  return wave_any(umax3(umax3(a[0], a[1], a[2]), umax3(a[3], b[0], b[1]), umax3(b[2], b[3], 0u)) == 0xffffffffu);
}
DEV u32x4 nofill(u32x4 v) {
#pragma unroll
  for (int i = 0; i < 4; ++i) v[i] = v[i] < 0xfffffffeu ? v[i] : 0xfffffffeu;
  return v;
}

#define WD 12
#define PD0 26
#define PD1 14
#define RSF 6
#define PD4 4
#define PD5 16
#define RSP 1
#define PACE0 2
#define PD2 16
#define RSG 8
#define TDS 3
#define NOACK 9

__global__ __launch_bounds__(512, 2) void fused_kernel(const void* __restrict__ Ax, const void* __restrict__ Aw, const void* __restrict__ Awo,
                                                       const void* __restrict__ Bx, const void* __restrict__ Bwq, const void* __restrict__ Bwk,
                                                       const void* __restrict__ Bwv, const void* __restrict__ Bwo, void* outp, void* wsp) {
  const int tid = __builtin_amdgcn_workitem_id_x(), lane = tid & 63;
  const int wid = __builtin_amdgcn_readfirstlane(tid >> 6);
  const int b = __builtin_amdgcn_workgroup_id_x(), xcd = b & 7, bi = b >> 3;
  const int rb = xcd * 4 + (bi >> 3), hg = bi & 7;
  const int kind = hg & 1, g16 = hg >> 1, g4 = lane >> 4;
  const unsigned vxx = (lane >> 3) * 8192 + (lane & 7) * 16;
  unsigned gen, vxb = (lane >> 3) * 1024 + (((lane & 7) ^ (lane >> 4)) << 4), nxb = 2 * g16;
  {
    dma_xb<2>(mkdesc(Bx, 2097152), vxb, rb * 65536u + wid * 128u, (wid >> 1) * STG + (wid & 1) * BLK, nxb);
    asm volatile("buffer_load_dword %0, off, %1, 0 sc1" : "=v"(gen) : "s"(mkdesc(wsp, WS_END)) : "memory");
    const u32x4 dw = mkdesc(kind ? Bwv : Bwk, 16384);
    unsigned so = wid * 2048 - 1024;
    sfor<2>([&](auto n) { dma<L_WKV + n * 1024>(dw, lane * 16, so, 1024, wid * 2048); });
    unsigned long long pc;
    int len;
    asm volatile("s_getpc_b64 %0\n.Lfk_here:\n\ts_mov_b32 %1, .Lfk_end-.Lfk_here" : "=s"(pc), "=s"(len));
    const u32x4 dc = {(unsigned)pc, (unsigned)(pc >> 32) & 0xffffu, (unsigned)(len > 0 ? len : 0), 0x00020000u};
    touch(dc, lane ? NONE : 0u, (wid * 32 + bi) * 128);
    touch(mkdesc(Bwq, 524288), lane < 16 ? lane * 1024u : NONE, ((bi >> 3) * 128 + wid * 16) * 1024 + hg * 128);
    SB();
  }
  const int rbA = (xcd >> 1) * 8 + (bi >> 2), hgA = (xcd & 1) * 4 + (bi & 3);
  const unsigned rotA = (bi >> 2) + 2 * (bi & 3);
  const rsrc_t ws = mkrsrc(wsp, WS_END);
  const int h = lane >> 5, blk = (lane >> 4) & 1, q4 = (lane & 15) >> 2, p4 = lane & 3;
  if (tid < 32) *(LAS unsigned*)(size_t)(L_CNT + 4 * tid) = 0;
  const int w = wid & 3, rh = w & 1, hp = w >> 1;
  unsigned cb = L_CNT, one = 1, bud = 1u << 22;
  PIN(cb); PIN(one);
  const unsigned wsw = ((lane & 7) ^ (((lane >> 4) & 1) << 2)) << 4;
  const unsigned wa = h * 1040 + q4 * 128 + (((hp * 4 + blk * 2 + (p4 >> 1)) ^ (((q4 >> 1) & 1) << 2)) << 4) + (p4 & 1) * 8;
  const unsigned lb = w * BLK;
  const rsrc_t out = mkrsrc(outp, 4194304);
  const int rg = rb * 2 + rh;
  unsigned obo = lane * 16 + WS_OB + (rg * 32 + hg * 4 + hp * 2) * 1024;

  bf16x8 wf[32];
  auto wbase = [&](unsigned* a, unsigned at) {
#pragma unroll
    for (int c = 0; c < 4; ++c) {
      a[c] = at + h * 2080 + (4 * blk + q4) * 128 + (((2 * c + (p4 >> 1)) ^ (((q4 >> 1) & 1) << 2) ^ (blk << 1)) << 4) + (p4 & 1) * 8;
      PIN(a[c]);
    }
  };
  auto wload = [&](unsigned at) {
    unsigned a[4];
    wbase(a, at);
#pragma unroll
    for (int i = 0; i < 32; ++i) wf[i] = ldstr(a[i & 3] + (i >> 2) * 4160, a[i & 3] + (i >> 2) * 4160 + 1040);
  };

  auto x_rest = [&](unsigned kb) { unsigned n = nxb; dma_xb<6>(mkdesc(Bx, 2097152), vxb, rb * 65536u + kb * 128u, (kb >> 1) * STG + (kb & 1) * BLK, n); };
  auto wq_block = [&](unsigned kb) {
    unsigned so = kb * 65536u + hg * 128u - 8192u;
    sfor<8>([&](auto n) { dma<2 * BLK + n * 1040>(mkdesc(Bwq, 524288), (lane >> 3) * 1024 + wsw, so, 8192, (kb >> 1) * STG + (kb & 1) * BLK); });
  };

  if (wid < 4) {
    __builtin_amdgcn_s_setprio(2);
    asm volatile("s_waitcnt vmcnt(2)" ::"v"(gen) : "memory");
    BARRIER();
    const unsigned qa = L_Q + w * 2048 + lane * 16;
    x_rest(w);
    wq_block(w);
    const unsigned vk = lane * 16;
    const unsigned vv = blk ? NONE : (unsigned)(h * 256 + (lane & 15) * 16);
    const unsigned cone = (lane & 27) == 16 ? 0x3f803f80u : 0u;
    u32x4 f0, f1;
    unsigned unsafe;
    PIN(obo);
    {
      unsigned sk = WS_KV + w * 16 * TS, lm = L_KVR + w * 12288, ka = lm + vk, va = lm + (vv & 1023);
      asm volatile(ATT_ASM
                   : "=&{v[216:219]}"(f0), "=&{v[232:235]}"(f1), "+s"(sk), "=&s"(unsafe), "+s"(lm), "+v"(ka), "+v"(va)
                   : "v"(vk), "v"(vv), "s"(mkdesc(wsp, WS_END)), "v"(cb), "v"(one),
                     "v"(qa), "v"(L_SCR + w * 1024 + (lane & 31) * 32), "v"(cb + 4 * w), "v"(cone),
                     "s"((L_KVR + w * 12288) ^ (L_KVR + w * 12288 + 6144)),
                     "v"(L_KVR + (w ^ 1) * 12288 + vk), "v"(L_KVR + (w ^ 2) * 12288 + 4096 + vk), "v"(L_KVR + (w ^ 3) * 12288 + 8192 + vk),
                     "v"((rh * 32 + (lane & 31)) * 128 + ((h ^ ((lane >> 1) & 7)) << 4)), "v"(wa + 2 * BLK)
                   : ATT_CLOB, "vcc", "scc", "memory"
                   );
    }
    if (__builtin_expect(unsafe != 0, 0)) {
      const f32x16 o0 = attn_true_max(ws, ldsr(qa), vk, vv, cone), o1 = attn_true_max(ws, ldsr(qa + 1024), vk, vv, cone);
      f0 = nofill(norm_frag(o0, o0[8]));
      f1 = nofill(norm_frag(o1, o1[8]));
    }
    st128(f0, ws, obo);
    st128(f1, ws, obo + 1024);
    *(volatile LAS unsigned*)(size_t)(L_CNT + C_TD + 4 * w) = 1;
    lds_wait<C_WB, 4, 1>(cb, bud);
    wload(0);
    __builtin_amdgcn_s_sleep(NOACK);
  } else {
    const int g = w;
    __builtin_amdgcn_s_setprio(1);
    u32x4 d = g == 0 ? mkdesc((const char*)Ax - 3120, 2097152 + 3120) : mkdesc((const char*)Aw - 3120, 1572864 + 3120);
    unsigned inc = g == 0 ? 1024u : 24576u, so, vp[4];
    const unsigned* vq;
    const unsigned sst = g == 0 ? 128u : 196608u, base = g == 0 ? rbA * 65536u : (g % 3) * 1024u + hgA * 128u;
#pragma unroll
    for (int n = 0; n < 4; ++n) { vp[n] = (g == 0 ? vxx : (lane >> 3) * 3072 + wsw) + n * inc + (3 - n) * 1040; PIN(vp[n]); }
    auto piece = [&](auto s_, auto n_) {
      constexpr int s = s_, n = n_;
      if constexpr (n == 0) {
        if constexpr (s < 8) so = base + ((s + rotA) & 7) * sst;
        else {
          inc = 8192;
          d = mkdesc((const char*)(s < 10 ? Awo : Bwo) - 3120, 524288 + 3120);
          so = (((s & 1) ^ (s < 10)) * 256 + g * 64) * 1024 + (s < 10 ? hgA : hg) * 128;
        }
      }
      if constexpr (n == 4) so += 4 * inc;
      if constexpr (n % 4 == 0) setm0<(s < 10 ? s % 3 : s & 1) * STG + n * 1040>(lb);
      dmi<(n % 4) * 1040>(d, s < 8 ? vp[n % 4] : vq[n % 4], so);
    };
    const int rga = rbA * 2 + rh, hd = hgA * 4 + g;
    unsigned oao = lane * 16 + WS_OA + (rga * 32 + hgA * 4 + hp * 2) * 1024;
    unsigned oas = WS_OA + (rbA * 64 + hd) * 1024 + (blk * 32 + h * 16 + (lane & 15)) * 16;
    unsigned pkv = WS_P + (hd * 32 + rbA) * PREC + lane * 16, pzz = lane < 16 ? WS_P + (hd * 32 + rbA) * PREC + 2048 + lane * 8 : NONE;
    unsigned pv = WS_P + (hd * 32 + (lane >> 3)) * PREC + (lane & 7) * 8 + rbA * 64;
    unsigned pz = WS_P + (hd * 32 + (lane >> 1)) * PREC + 2048 + (rbA & 7) * 16 + (lane & 1) * 8;
    unsigned rkv = WS_R + hd * 4096 + (lane >> 2) * 128 + p4 * 8, rzz = WS_R + hd * 4096 + g4 * 256 + 32;
    unsigned pr_at = lane <= 8 && !(lane & 1) ? WS_R + hd * 4096 + rbA * 128 + lane * 4 : NONE;
    unsigned fcs = lane < 2 ? WS_FC + (lane ? rga * 32 + 16 + hgA * 2 + hp : rg * 32 + hg * 2 + hp) * FS : NONE;
    unsigned fc = WS_FC + ((blk ? rbA * 2 + hp : rg) * 32 + (lane & 31)) * FS;
    unsigned vpo[4];
#pragma unroll
    for (int i = 0; i < 4; ++i) { vpo[i] = (lane >> 3) * 1024 + (wsw ^ (h << 5)) + i * 8192 + (3 - i) * 1040; PIN(vpo[i]); }
    PIN(oao); PIN(obo); PIN(oas); PIN(pkv); PIN(pzz); PIN(pv); PIN(pz); PIN(rkv); PIN(rzz); PIN(pr_at); PIN(fcs); PIN(fc);
    const unsigned xa16 = (lane & 7) * 1040 + ((lane >> 3) & 1) * 128 + g4 * 16;
    const unsigned wa16 = BLK + g4 * 1040 + q4 * 128 + (((g * 2 + (p4 >> 1)) ^ (((q4 >> 1) & 1) << 2)) << 4) + (p4 & 1) * 8;
    vq = vpo;
    unsigned epoch;
    {
      unsigned ux = (g16 * 16 + (lane & 15)) * 128 + ((g4 ^ ((lane & 15) >> 1)) << 4), ux1 = ux ^ 64, ux2 = ux + 2 * STG, ux3 = ux1 + 2 * STG;
      unsigned uw = L_WKV + (8 * g4 + q4) * 32 + p4 * 8;
      unsigned off = WS_KV + (rb * 2 + (g16 >> 1)) * TS +
                     (kind ? 1024 + (g16 & 1) * 512 + (g4 >> 1) * 256 + (lane & 15) * 16 + (g4 & 1) * 8
                           : ((g4 & 1) * 32 + (g16 & 1) * 16 + (lane & 15)) * 16 + (g4 >> 1) * 8);
      PIN(ux); PIN(ux1); PIN(ux2); PIN(ux3); PIN(uw); PIN(off);
      asm volatile("s_waitcnt vmcnt(2)\n\tv_readfirstlane_b32 %0, %1" : "=s"(epoch) : "v"(gen) : "memory");
      epoch = epoch + 1u ? epoch + 1u : 1u;
      BARRIER();
      if (g == 0) {
        __builtin_amdgcn_s_setprio(3);
        f32x4 u0 = {}, u1 = {};
        bf16x8 a[16], c[16];
#pragma unroll
        for (int s = 0; s < 16; ++s) {
          a[s] = ldsr((s & 1 ? (s < 8 ? ux1 : ux3) : (s < 8 ? ux : ux2)) + ((s >> 2) & 1) * STG + ((s >> 1) & 1) * BLK);
          c[s] = ldstr(uw + s * 1024, uw + s * 1024 + 128);
        }
        SB();
        if (kind) {
#pragma unroll
          for (int s = 0; s < 16; s += 2) { u0 = MFMA16(a[s], c[s], u0); u1 = MFMA16(a[s + 1], c[s + 1], u1); }
        } else {
#pragma unroll
          for (int s = 0; s < 16; s += 2) { u0 = MFMA16(c[s], a[s], u0); u1 = MFMA16(c[s + 1], a[s + 1], u1); }
        }
        u0 += u1;
        __builtin_amdgcn_raw_buffer_store_b64((u32x2){pk2(u0[0], u0[1]), pk2(u0[2], u0[3])}, ws, off, 0, 16);
        lds_signal<C_T>(cb, one);
        publish(ws, WS_FKV + (rb * 8 + hg) * FS, epoch, lane);
        __builtin_amdgcn_s_sleep(PD0);
      } else {
        __builtin_amdgcn_s_sleep(WD);
#pragma unroll 1
        for (int kb = 3 + g; kb < 8; kb += 3) wq_block(kb);
        if (g > 1) {
#pragma unroll 1
          for (int kb = 2 + g; kb < 8; kb += 2) x_rest(kb);
        }
        VMCNT(0);
        lds_signal<C_W + 4>(cb, one);
        __builtin_amdgcn_s_sleep(PD1);
      }
    }
    unsigned epv = epoch;
    PIN(epv);
    {
      const unsigned fkv = WS_FKV + (g * 64 + lane) * FS;
      __builtin_amdgcn_s_setprio(3);
      wait_flags<RSF>(ws, fkv, ld32(ws, fkv, 0), epoch);
      lds_signal<C_KVR>(cb + 4 * g, one);
    }
    lds_wait<C_T, 5, 1>(cb, bud);
    {
      const u32x4 dk = mkdesc(wsp, WS_END);
      __builtin_amdgcn_s_sleep(PD4);
      sfor<3>([&](auto r) {
        setm0<L_KVR + r * 4096>(g * 12288);
        sfor<4>([&](auto n) { dmi_dev<n * 1024>(dk, lane * 16, WS_KV + (g * 16 + 1) * TS + r * 4096); });
      });
    }
    lds_signal<C_S>(cb, one);
    VMCNT(0);
    lds_signal<C_KVR>(cb + 4 * g, one);
    lds_wait<C_S, 4, 1>(cb, bud);
    sfor<24>([&](auto n) { piece(IC(n / 8), IC(n % 8)); });
    if (g < 2) touch(mkdesc(g ? Bwo : Awo, 524288), lane < 16 ? lane * 128u : NONE, b * 2048);
    f32x4 dk[4] = {}, dv[4] = {}, dq[4] = {};
    {
      bf16x8 xf[2][2][4], wv[2][2][3];
      auto mm = [&](auto p_, auto i_) {
        constexpr int p = p_, t = i_ / 12, c = i_ % 12 / 4, r = i_ % 4;
        if constexpr (c == 0) dk[r] = MFMA16(xf[p][t][r], wv[p][t][0], dk[r]);
        if constexpr (c == 1) dv[r] = MFMA16(xf[p][t][r], wv[p][t][1], dv[r]);
        if constexpr (c == 2) dq[r] = MFMA16(wv[p][t][2], xf[p][t][r], dq[r]);
      };
      VMCNT(16);
      lds_signal<C_G>(cb, one);
      sfor<8>([&](auto s_) {
        constexpr int s = s_, cur = s & 1, prv = cur ^ 1;
        unsigned bx = xa16 + (s % 3) * STG, bw = wa16 + (s % 3) * STG;
        PIN(bx); PIN(bw);
        lds_wait<C_G, 4 * s + 4, (s == 0 ? 2 : 0)>(cb, bud);
        if constexpr (s == 0) { if (b == 0 && tid == 256) __builtin_amdgcn_raw_buffer_store_b32(epoch, ws, WS_GEN, 0, 16); }
        auto rd = [&](auto i_) {
          constexpr int t = i_ / 7, c = i_ % 7;
          if constexpr (c < 4) xf[cur][t][c] = ldsr(bx + c * 256 + t * 64);
          else wv[cur][t][c - 4] = ldstr(bw + (c - 4) * BLK + t * 4160, bw + (c - 4) * BLK + t * 4160 + 512);
        };
        sfor<4>([&](auto i_) {
          constexpr int i = i_;
          if constexpr (s == 0 && i == 0) __builtin_amdgcn_s_setprio(1);
          if constexpr (i < 3) sfor<(i < 2 ? 5 : 4)>([&](auto r) { rd(IC(5 * i + r)); });
          SB();
          if constexpr (s > 0 && i < 3) sfor<(i < 2 ? 3 : 2)>([&](auto r) { piece(IC(s + 2), IC(3 * i + r)); });
          if constexpr (s < 7 && i == 3) { VMCNT(8); lds_signal<C_G>(cb, one); }
          if constexpr (i == 3 && s >= 2 && s < 6) { st128((u32x4){~0u, ~0u, ~0u, ~0u}, ws, (s < 4 ? obo : oao) + (s & 1) * 1024); SB(); }
          SB();
          if constexpr (s > 0) sfor<6>([&](auto r) { mm(IC(prv), IC(6 * i + r)); });
          SB();
        });
#pragma unroll
        for (int r = 0; r < 4; ++r) { PIN(dk[r]); PIN(dv[r]); PIN(dq[r]); }
      });
      sfor<24>([&](auto r) { mm(IC(1), r); });
    }
    {
      const bf16x8 ones = asfrag(0x3f803f80u, 0x3f803f80u, 0x3f803f80u, 0x3f803f80u);
      f32x4 d2 = {}, dz = {};
#pragma unroll
      for (int s = 0; s < 4; s += 2) {
        bf16x8 fk = asfrag(phi2(dk[s][0], dk[s][1]), phi2(dk[s][2], dk[s][3]), phi2(dk[s + 1][0], dk[s + 1][1]), phi2(dk[s + 1][2], dk[s + 1][3]));
        bf16x8 fv = asfrag(pk2(dv[s][0], dv[s][1]), pk2(dv[s][2], dv[s][3]), pk2(dv[s + 1][0], dv[s + 1][1]), pk2(dv[s + 1][2], dv[s + 1][3]));
        d2 = MFMA16(fk, fv, d2);
        dz = MFMA16(ones, fk, dz);
      }
      PIN(d2); PIN(dz);
      VMCNT(0);
      __builtin_amdgcn_raw_buffer_store_b32(epv, ws, fcs, 0, 16);
      lds_signal<C_G>(cb, one);
#pragma unroll
      for (int i = 0; i < 4; i += 2) {
        const unsigned x = fbits(d2[i]), y = fbits(d2[i + 1]);
        st128((u32x4){x, x ^ epoch, y, y ^ epoch}, ws, pkv + i * 512);
      }
      const unsigned z = fbits(dz[0]);
      __builtin_amdgcn_raw_buffer_store_b64((u32x2){z, z ^ epoch}, ws, pzz, 0, 16);
    }
    auto reduce = [&]() {
      u32x2 x[5];
#pragma unroll 1
      for (unsigned s = 0; s < SPIN_CAP; ++s) {
        unsigned bad = 0;
#pragma unroll
        for (int i = 0; i < 5; ++i) x[i] = __builtin_amdgcn_raw_buffer_load_b64(ws, i < 4 ? pv : pz, i < 4 ? i * 8 * PREC : 0, 16);
#pragma unroll
        for (int i = 0; i < 5; ++i) { unsigned t = x[i][0] ^ x[i][1] ^ epoch; PIN(t); bad |= t; }
        if (!wave_any(bad != 0)) break;
        __builtin_amdgcn_s_sleep(RSP);
        asm volatile("" ::: "memory");
      }
      float v = (asf(x[0][0]) + asf(x[1][0])) + (asf(x[2][0]) + asf(x[3][0])), z = asf(x[4][0]);
      z += dpp<0x4E>(z);
      z += dpp<0x124>(z);
      v += dpp<0x128>(v); z += dpp<0x128>(z);
      v = (lane & 8) ? z : v;
      float t = v;
      PIN(t); swap16(v, t); v += t; t = v;
      PIN(t); swap32(v, t); v += t;
      const unsigned pr = pk2(v, dpp<0xB1>(v));
      __builtin_amdgcn_raw_buffer_store_b64((u32x2){pr, pr ^ epoch}, ws, pr_at, 0, 16);
    };
    u32x2 pq[4];
#pragma unroll
    for (int r = 0; r < 4; ++r) { pq[r] = (u32x2){phi2(dq[r][0], dq[r][1]), phi2(dq[r][2], dq[r][3])}; PIN(pq[r]); }
    lds_wait<C_G, 36, 0>(cb, bud);
    wload(0);
    lds_signal<C_G>(cb, one);
    __builtin_amdgcn_s_sleep(PD5);
    reduce();
    {
      const unsigned pc = ld32(ws, fc, 0);
      lds_wait<C_G, 40, 1>(cb, bud);
      sfor<2>([&](auto s) { sfor<8>([&](auto n) { piece(IC(s + 10), n); __builtin_amdgcn_s_sleep(PACE0); }); });
      wait_flags<8>(ws, fc, pc, epoch);
      lds_signal<C_RF>(cb, one);
    }
    {
      u32x2 x[4];
#pragma unroll 1
      for (unsigned s = 0; s < SPIN_CAP; ++s) {
#pragma unroll
        for (int i = 0; i < 4; ++i) x[i] = __builtin_amdgcn_raw_buffer_load_b64(ws, i < 2 ? rkv + i * 2048 : rzz + (i - 2) * 128, 0, 16);
        unsigned bad = 0;
#pragma unroll
        for (int i = 0; i < 4; ++i) { unsigned t = x[i][0] ^ x[i][1] ^ epoch; PIN(t); bad |= t; }
        if (!wave_any(bad != 0)) break;
        __builtin_amdgcn_s_sleep(RSP);
        asm volatile("" ::: "memory");
      }
      const s16x4 kvf = __builtin_bit_cast(s16x4, (u32x2){x[0][0], x[1][0]}), zf = __builtin_bit_cast(s16x4, (u32x2){x[2][0], x[3][0]});
      float y[4][2];
#pragma unroll
      for (int r = 0; r < 4; ++r) {
        const s16x4 p = __builtin_bit_cast(s16x4, pq[r]);
        const f32x4 num = __builtin_amdgcn_mfma_f32_16x16x16bf16_1k(kvf, p, (f32x4){}, 0, 0, 0);
        const f32x4 den = __builtin_amdgcn_mfma_f32_16x16x16bf16_1k(zf, p, (f32x4){}, 0, 0, 0);
        const float rc = __builtin_amdgcn_rcpf(den[0] + 1e-6f);
        y[r][0] = asf(pk2(num[0] * rc, num[1] * rc)); y[r][1] = asf(pk2(num[2] * rc, num[3] * rc));
      }
#pragma unroll
      for (int r = 0; r < 4; r += 2) {
        swap32(y[r][0], y[r + 1][0]); swap32(y[r][1], y[r + 1][1]);
        st128(nofill((u32x4){fbits(y[r][0]), fbits(y[r][1]), fbits(y[r + 1][0]), fbits(y[r + 1][1])}), ws, oas + r * 16384);
      }
    }
    lds_signal<C_WB>(cb, one);
    VMCNT(0);
    __builtin_amdgcn_s_sleep(PD2);
  }
  {
    const bool isT = wid < 4;
    bud = 1u << 22;
    const int ro = (isT ? rb : rbA) * 2 + hp, co = isT ? 512 + hg * 64 : hgA * 64;
    const unsigned o0 = (isT ? WS_OB : WS_OA) + ro * 32768, ov = h * 1024 + (blk * 32 + rh * 16 + (lane & 15)) * 16;
    u32x4 of[16];
    auto oload = [&](int j) { of[j] = ld128(ws, ov + (j & 1) * 2048, o0 + (j >> 1) * 4096); };
#pragma unroll
    for (int j = 0; j < 16; ++j) oload(j);
    SB();
    unsigned ob = ((ro * 32 + rh * 16 + (lane & 15)) * 1024 + co + blk * 16 + h * 8) * 2, a2[4];
    PIN(ob);
    wbase(a2, isT ? STG : 2 * STG);
    auto stale = [&](int r) { return cleared(of[4 * r], of[4 * r + 1]) | cleared(of[4 * r + 2], of[4 * r + 3]); };
    if (!isT) {
#pragma unroll 1
      for (unsigned s = 0; s < SPIN_CAP; ++s) {
        bool again[4];
#pragma unroll
        for (int r = 0; r < 4; ++r) again[r] = wave_any(umax3(umax3(of[4 * r][3], of[4 * r + 1][3], of[4 * r + 2][3]), of[4 * r + 3][3], 0u) == 0xffffffffu);
        if (!(again[0] | again[1] | again[2] | again[3])) break;
        __builtin_amdgcn_s_sleep(RSG);
        asm volatile("" ::: "memory");
#pragma unroll
        for (int r = 0; r < 4; ++r)
          if (again[r]) {
#pragma unroll
            for (int j = 4 * r; j < 4 * r + 4; ++j) oload(j);
          }
      }
      lds_wait<C_TD, 1, TDS>(cb + 4 * w, bud);
      __builtin_amdgcn_s_setprio(0);
    }
    f32x4 c[4] = {};
    auto top = [&](int j, unsigned m) { return umax3(umax3(of[j][0], of[j][1], of[j][2]), of[j][3], m); };
    unsigned m = top(3, top(2, top(1, top(0, 0u))));
    sfor<4>([&](auto p_) {
      constexpr int p = p_;
#pragma unroll 1
      for (unsigned s = 0; s < SPIN_CAP && wave_any(m == 0xffffffffu); ++s) {
        bool again[4];
#pragma unroll
        for (int r = p + 1; r < 4; ++r) again[r] = stale(r);
        asm volatile("" ::: "memory");
#pragma unroll
        for (int r = p; r < 4; ++r)
          if (r == p || again[r]) {
#pragma unroll
            for (int j = 4 * r; j < 4 * r + 4; ++j) oload(j);
          }
        m = top(4 * p + 3, top(4 * p + 2, top(4 * p + 1, top(4 * p, 0u))));
      }
      m = 0;
#pragma unroll
      for (int j = 4 * p; j < 4 * p + 4; ++j) {
#pragma unroll
        for (int i = 0; i < 4; ++i) {
          c[i] = MFMA16(wf[4 * (j & 7) + i], asfrag(of[j]), c[i]);
          SB();
          if constexpr (p < 2) { wf[4 * j + i] = ldstr(a2[i] + j * 4160, a2[i] + j * 4160 + 1040); SB(); }
        }
      }
      if constexpr (p < 3) m = top(4 * p + 7, top(4 * p + 6, top(4 * p + 5, top(4 * p + 4, 0u))));
#pragma unroll
      for (int i = 0; i < 4; ++i) PIN(c[i]);
      PIN(m);
    });
#pragma unroll
    for (int i = 0; i < 4; i += 2) {
      float x0 = asf(pk2(c[i][0], c[i][1])), x1 = asf(pk2(c[i][2], c[i][3])), y0 = asf(pk2(c[i + 1][0], c[i + 1][1])), y1 = asf(pk2(c[i + 1][2], c[i + 1][3]));
      swap16(x0, y0); swap16(x1, y1);
      __builtin_amdgcn_raw_buffer_store_b128((u32x4){fbits(x0), fbits(x1), fbits(y0), fbits(y1)}, out, ob + i * 32, 0, 16);
    }
    asm volatile(".Lfk_end:");
  }
}
#if defined(__HIP_DEVICE_COMPILE__)
#pragma clang attribute pop
#endif

extern "C" void kernel_launch(void* const* d_in, const int* in_sizes, int n_in, void* d_out, int out_size, void* d_ws, size_t ws_size,
                              hipStream_t stream) {
  (void)hipFuncSetAttribute((const void*)fused_kernel, hipFuncAttributeMaxDynamicSharedMemorySize, LDS_BYTES);
  fused_kernel<<<256, 512, LDS_BYTES, stream>>>(d_in[0], d_in[1], d_in[2], d_in[3], d_in[4], d_in[5], d_in[6], d_in[7], d_out, d_ws);
}
```
